```python
import torch

def _softmax(x, axis=-1):
    return torch.softmax(x, dim=axis)
def _gelu(x):
    return torch.nn.functional.gelu(x, approximate="tanh")
def _silu(x):
    return torch.nn.functional.silu(x)
def _sigmoid(x):
    return torch.sigmoid(x)
def _layernorm(x, axis=-1, eps=1e-5):
    m = x.mean(dim=axis, keepdim=True)
    v = x.var(dim=axis, keepdim=True, unbiased=False)
    return (x - m) / torch.sqrt(v + eps)
def _scatter_add(tgt, idx, src, axis=0):
    return tgt.clone().index_add_(axis, idx, src)
def _segment_sum(src, seg, n):
    out = torch.zeros((n,) + src.shape[1:], dtype=src.dtype)
    return out.index_add_(0, seg, src)
def _pad(x, pw):
    flat = tuple(v for pair in reversed(pw) for v in pair)
    return torch.nn.functional.pad(x, flat)

kernel_name = 'interleave_layers__mha_block__glu_gated_glu__prompt'

def setup_inputs():

    _w = (lambda a: (lambda t: t.to(torch.bfloat16) if t.is_floating_point() else t)(torch.as_tensor(a)))
    def _A_setup():
        B, S, D, H = 1, 2048, 448, 16
        return {
            "x": _w((lambda *s: torch.randn(*s, dtype=torch.bfloat16) * 0.05)(B, S, D)),
            "wqkv": _w((lambda *s: torch.randn(*s, dtype=torch.bfloat16) * 0.05)(D, 3 * D)),
            "wo": _w((lambda *s: torch.randn(*s, dtype=torch.bfloat16) * 0.05)(D, D)),
        }
    _A_in = _A_setup()

    def _B_setup():
        B, S, D, F = 1, 2048, 448, 1792
        return {
            "x": _w((lambda *s: torch.randn(*s, dtype=torch.bfloat16) * 0.05)(B, S, D)),
            "w_gate": _w((lambda *s: torch.randn(*s, dtype=torch.bfloat16) * 0.05)(D, F)),
            "w_up": _w((lambda *s: torch.randn(*s, dtype=torch.bfloat16) * 0.05)(D, F)),
            "w_down": _w((lambda *s: torch.randn(*s, dtype=torch.bfloat16) * 0.05)(F, D)),
        }
    _B_in = _B_setup()

    return {
        "A_x": _A_in["x"],
        "A_wqkv": _A_in["wqkv"],
        "A_wo": _A_in["wo"],
        "B_x": _B_in["x"],
        "B_w_gate": _B_in["w_gate"],
        "B_w_up": _B_in["w_up"],
        "B_w_down": _B_in["w_down"],
    }

def reference(A_x, A_wqkv, A_wo, B_x, B_w_gate, B_w_up, B_w_down):
    def _A_ref(x, wqkv, wo):
        B, S, D = x.shape
        H = 16
        Dh = D // H
        qkv = (lambda x, *d: x.permute(*d))((x @ wqkv).reshape(B, S, 3, H, Dh), 2, 0, 3, 1, 4)
        q, k, v = qkv[0], qkv[1], qkv[2]
        scores = torch.einsum("bhqd,bhkd->bhqk", q, k) * (Dh ** -0.5)
        p = _softmax(scores, axis=-1)
        o = torch.einsum("bhqk,bhkd->bhqd", p, v)
        return (lambda x, *d: x.permute(*d))(o, 0, 2, 1, 3).reshape(B, S, D) @ wo

    def _B_ref(x, w_gate, w_up, w_down):
        g = torch.einsum("bsd,df->bsf", x, w_gate)
        u = torch.einsum("bsd,df->bsf", x, w_up)
        h = _sigmoid(g) * u
        return torch.einsum("bsf,fd->bsd", h, w_down)

    x = A_x
    x = x + _A_ref(_layernorm(x, axis=-1), A_wqkv, A_wo)
    x = x + _B_ref(_layernorm(x, axis=-1), B_w_gate, B_w_up, B_w_down)
    x = x + _A_ref(_layernorm(x, axis=-1), A_wqkv, A_wo)
    x = x + _B_ref(_layernorm(x, axis=-1), B_w_gate, B_w_up, B_w_down)
    return x
```

```cpp
#include <hip/hip_runtime.h>
#include <hip/hip_bf16.h>
#include <stdint.h>

constexpr int S_ = 2048, D_ = 448, H_ = 16, DH_ = 28, F_ = 1792;

typedef unsigned short u16;
typedef __attribute__((ext_vector_type(8))) short bf16x8;
typedef __attribute__((ext_vector_type(4))) short s16x4;
typedef __attribute__((ext_vector_type(16))) float f32x16;
typedef __attribute__((ext_vector_type(8))) float f32x8;
typedef __attribute__((ext_vector_type(4))) float f32x4;
typedef __attribute__((ext_vector_type(4))) unsigned u32x4;
typedef __attribute__((ext_vector_type(2))) unsigned u32x2;
typedef __attribute__((ext_vector_type(8))) __bf16 bf8;
typedef __attribute__((ext_vector_type(4))) __bf16 bf4;
typedef __attribute__((ext_vector_type(2))) __bf16 bf2;
typedef __attribute__((address_space(3))) s16x4* lds_s16x4_p;

#define DI __device__ __forceinline__
#define MFMA32(a, b, c) __builtin_amdgcn_mfma_f32_32x32x16_bf16((a), (b), (c), 0, 0, 0)

DI int crow(int reg, int h) { return (reg & 3) + 8 * (reg >> 2) + 4 * h; }

DI s16x4 tr_read(const char* p) {
#if defined(__HIP_DEVICE_COMPILE__)
  return __builtin_amdgcn_ds_read_tr16_b64_v4i16((lds_s16x4_p)(p));
#else
  return s16x4{};
#endif
}
DI bf16x8 cat(s16x4 lo, s16x4 hi) { return __builtin_shufflevector(lo, hi, 0, 1, 2, 3, 4, 5, 6, 7); }
DI bf16x8 pack8(f32x8 v) { return __builtin_bit_cast(bf16x8, __builtin_convertvector(v, bf8)); }
DI u16 f2bf(float f) { return __builtin_bit_cast(u16, (__bf16)f); }
DI float bf2f(u16 u) { return __uint_as_float(((unsigned)u) << 16); }
DI float wave_sum(float v) {
#pragma unroll
  for (int o = 32; o >= 1; o >>= 1) v += __shfl_xor(v, o, 64);
  return v;
}

DI void load8(const float* p, float* v) {
  f32x4 a = *(const f32x4*)p, b = *(const f32x4*)(p + 4);
  v[0] = a[0]; v[1] = a[1]; v[2] = a[2]; v[3] = a[3]; v[4] = b[0]; v[5] = b[1]; v[6] = b[2]; v[7] = b[3];
}
DI void load8(const u16* p, float* v) {
  u32x4 a = *(const u32x4*)p;
#pragma unroll
  for (int i = 0; i < 4; ++i) { v[2 * i] = __uint_as_float(a[i] << 16); v[2 * i + 1] = __uint_as_float(a[i] & 0xffff0000u); }
}

constexpr int ASTR = 912;

template <typename TX, int NROWS, int NW>
DI void ln_rows_to_lds(const TX* __restrict__ x, int row0, char* sA, int wid, int lane) {
  constexpr int RPW = NROWS / NW;
#pragma unroll 4
  for (int i = 0; i < RPW; ++i) {
    const int r = wid * RPW + i;
    float v[8];
    if (lane < 56) load8(x + (size_t)(row0 + r) * D_ + lane * 8, v);
    else {
#pragma unroll
      for (int j = 0; j < 8; ++j) v[j] = 0.f;
    }
    float s = 0.f;
#pragma unroll
    for (int j = 0; j < 8; ++j) s += v[j];
    s = wave_sum(s);
    const float mean = s * (1.f / 448.f);
    float q = 0.f;
    if (lane < 56) {
#pragma unroll
      for (int j = 0; j < 8; ++j) { float d = v[j] - mean; q += d * d; }
    }
    q = wave_sum(q);
    const float rstd = rsqrtf(q * (1.f / 448.f) + 1e-5f);
    if (lane < 56) {
      f32x8 o;
#pragma unroll
      for (int j = 0; j < 8; ++j) o[j] = (v[j] - mean) * rstd;
      *(bf16x8*)(sA + r * ASTR + lane * 16) = pack8(o);
    }
  }
}

template <typename TX>
__global__ __launch_bounds__(256) void k_ln_qkv(const TX* __restrict__ x, const u16* __restrict__ w, u16* __restrict__ qkvp) {
  extern __shared__ __attribute__((aligned(16))) char smem[];
  constexpr int BSTR = 448;
  char* sA = smem;
  char* sB = smem + 64 * ASTR;
  const int tid = threadIdx.x, wid = tid >> 6, lane = tid & 63;
  const int mt = blockIdx.x / 7, nt = blockIdx.x % 7;
  const int r32 = lane & 31, h = lane >> 5;
  const int wm = wid & 1, wn = wid >> 1;

  u32x4 st[6];
  auto gload = [&](int kc) {
#pragma unroll
    for (int i = 0; i < 6; ++i) {
      int p = tid + 256 * i; int row = p / 24, c = p % 24;
      st[i] = *(const u32x4*)(w + (size_t)(kc * 64 + row) * 1344 + nt * 192 + c * 8);
    }
  };
  auto swrite = [&](int buf) {
#pragma unroll
    for (int i = 0; i < 6; ++i) {
      int p = tid + 256 * i; int row = p / 24, c = p % 24;
      *(u32x4*)(sB + buf * (64 * BSTR) + row * BSTR + c * 16) = st[i];
    }
  };
  gload(0);
  ln_rows_to_lds<TX, 64, 4>(x, mt * 64, sA, wid, lane);
  swrite(0);
  __syncthreads();

  f32x16 acc[3];
#pragma unroll
  for (int t = 0; t < 3; ++t)
#pragma unroll
    for (int i = 0; i < 16; ++i) acc[t][i] = 0.f;

  const int i16 = lane & 15, q = i16 >> 2, p4 = i16 & 3, blk = (lane >> 4) & 1;
  const int boff = (8 * h + q) * BSTR + (wn * 96 + 16 * blk + 4 * p4) * 2;
  const int aoff = (wm * 32 + r32) * ASTR + 16 * h;
  for (int kc = 0; kc < 7; ++kc) {
    if (kc < 6) gload(kc + 1);
    const char* bb = sB + (kc & 1) * (64 * BSTR) + boff;
#pragma unroll
    for (int ks = 0; ks < 4; ++ks) {
      bf16x8 a = *(const bf16x8*)(sA + aoff + (kc * 64 + ks * 16) * 2);
#pragma unroll
      for (int t = 0; t < 3; ++t) {
        s16x4 lo = tr_read(bb + ks * 16 * BSTR + t * 64);
        s16x4 hi = tr_read(bb + ks * 16 * BSTR + 4 * BSTR + t * 64);
        acc[t] = MFMA32(a, cat(lo, hi), acc[t]);
      }
    }
    if (kc < 6) swrite((kc + 1) & 1);
    __syncthreads();
  }
  constexpr int CSTR = 400;
  char* sC = smem;
  const float SC = 0.18898223650461362f * 1.4426950408889634f;
#pragma unroll
  for (int t = 0; t < 3; ++t) {
    const int col = wn * 96 + t * 32 + r32;
    const float sc = (nt * 192 + col) < 448 ? SC : 1.f;
#pragma unroll
    for (int i = 0; i < 16; ++i) {
      const int row = wm * 32 + crow(i, h);
      *(u16*)(sC + row * CSTR + col * 2) = f2bf(acc[t][i] * sc);
    }
  }
  __syncthreads();
#pragma unroll
  for (int i = 0; i < 12; ++i) {
    const int id = tid + 256 * i; const int row = id / 48, pc = id % 48;
    const int n = nt * 192 + pc * 4;
    const int sec = n / 448, rem = n % 448, head = rem / 28, d0 = rem % 28;
    u32x2 v = *(const u32x2*)(sC + row * CSTR + pc * 8);
    u16* dst = qkvp + ((size_t)(sec * 16 + head) * 2048 + (mt * 64 + row)) * 32 + d0;
    *(u32x2*)dst = v;
    if (d0 == 24) {
      u32x2 pad; pad[0] = (sec == 2) ? 0x00003F80u : 0u; pad[1] = 0u;
      *(u32x2*)(dst + 4) = pad;
    }
  }
}

__global__ __launch_bounds__(512) void k_attn(const u16* __restrict__ qkvp, u16* __restrict__ o_out) {
  extern __shared__ __attribute__((aligned(16))) char smem[];
  const int tid = threadIdx.x, wid = tid >> 6, lane = tid & 63;
  const int head = blockIdx.x >> 4, qb = blockIdx.x & 15;
  const int qt = wid & 3, kh = wid >> 2;
  const int r32 = lane & 31, h = lane >> 5;
  const u16* Q = qkvp + (size_t)(head) * 2048 * 32;
  const u16* K = qkvp + (size_t)(16 + head) * 2048 * 32;
  const u16* V = qkvp + (size_t)(32 + head) * 2048 * 32;
  const int qrow = qb * 128 + qt * 32 + r32;
  const bf16x8 q0 = *(const bf16x8*)(Q + (size_t)qrow * 32 + 8 * h);
  const bf16x8 q1 = *(const bf16x8*)(Q + (size_t)qrow * 32 + 16 + 8 * h);
  const int shalf = tid >> 8, st = tid & 255, skey = st >> 2, sslot = st & 3;
  const u16* gK = K + (size_t)(shalf * 1024 + skey) * 32 + sslot * 8;
  const u16* gV = V + (size_t)(shalf * 1024 + skey) * 32 + sslot * 8;
  char* sbase_st = smem + shalf * 16384;
  const int kst_off = skey * 64 + ((sslot ^ ((skey >> 2) & 3)) * 16);
  const int vst_off = 4096 + skey * 64 + sslot * 16;
  u32x4 rk, rv;
  rk = *(const u32x4*)gK; rv = *(const u32x4*)gV;
  *(u32x4*)(sbase_st + kst_off) = rk; *(u32x4*)(sbase_st + vst_off) = rv;
  __syncthreads();

  const char* sbase = smem + kh * 16384;
  const int swz = (r32 >> 2) & 3;
  const int ka_off0 = r32 * 64 + ((h ^ swz) * 16);
  const int ka_off1 = r32 * 64 + (((2 + h) ^ swz) * 16);
  const int i16 = lane & 15, q4 = i16 >> 2, p4 = i16 & 3, blk = (lane >> 4) & 1;
  const int v_off = 4096 + (4 * h + q4) * 64 + (16 * blk + 4 * p4) * 2;

  f32x16 o, cneg;
#pragma unroll
  for (int i = 0; i < 16; ++i) { o[i] = 0.f; cneg[i] = 0.f; }
  float m = 0.f;
  const unsigned long long ones = ~0ull;
  for (int it = 0; it < 16; ++it) {
    if (it < 15) {
      rk = *(const u32x4*)(gK + (size_t)(it + 1) * 64 * 32);
      rv = *(const u32x4*)(gV + (size_t)(it + 1) * 64 * 32);
    }
    const char* sb = sbase + (it & 1) * 8192;
    bf16x8 ka0 = *(const bf16x8*)(sb + ka_off0);
    bf16x8 ka1 = *(const bf16x8*)(sb + ka_off1);
    bf16x8 kb0 = *(const bf16x8*)(sb + 2048 + ka_off0);
    bf16x8 kb1 = *(const bf16x8*)(sb + 2048 + ka_off1);
    f32x16 s0 = MFMA32(ka0, q0, cneg); s0 = MFMA32(ka1, q1, s0);
    f32x16 s1 = MFMA32(kb0, q0, cneg); s1 = MFMA32(kb1, q1, s1);
    float mx = fmaxf(s0[0], s1[0]);
#pragma unroll
    for (int i = 1; i < 16; ++i) mx = fmaxf(mx, fmaxf(s0[i], s1[i]));
    {
      auto rr = __builtin_amdgcn_permlane32_swap(__float_as_uint(mx), __float_as_uint(mx), false, false);
      mx = fmaxf(__uint_as_float(rr[0]), __uint_as_float(rr[1]));
    }
    if (it == 0 || __any(mx > 0.f)) {
      const float d = (it == 0) ? mx : fmaxf(mx, 0.f);
      const float alpha = (it == 0) ? 1.f : __builtin_amdgcn_exp2f(-d);
#pragma unroll
      for (int i = 0; i < 16; ++i) { o[i] *= alpha; s0[i] -= d; s1[i] -= d; }
      m += d;
#pragma unroll
      for (int i = 0; i < 16; ++i) cneg[i] = -m;
    }
    f32x8 e0, e1, e2, e3;
#pragma unroll
    for (int i = 0; i < 8; ++i) {
      e0[i] = __builtin_amdgcn_exp2f(s0[i]); e1[i] = __builtin_amdgcn_exp2f(s0[8 + i]);
      e2[i] = __builtin_amdgcn_exp2f(s1[i]); e3[i] = __builtin_amdgcn_exp2f(s1[8 + i]);
    }
    bf16x8 p0 = pack8(e0), p1 = pack8(e1), p2 = pack8(e2), p3 = pack8(e3);
    const char* vb = sb + v_off;
    o = MFMA32(cat(tr_read(vb), tr_read(vb + 8 * 64)), p0, o);
    o = MFMA32(cat(tr_read(vb + 16 * 64), tr_read(vb + 24 * 64)), p1, o);
    o = MFMA32(cat(tr_read(vb + 32 * 64), tr_read(vb + 40 * 64)), p2, o);
    o = MFMA32(cat(tr_read(vb + 48 * 64), tr_read(vb + 56 * 64)), p3, o);
    if (it < 15) {
      char* sw = sbase_st + ((it + 1) & 1) * 8192;
      *(u32x4*)(sw + kst_off) = rk; *(u32x4*)(sw + vst_off) = rv;
    }
    __syncthreads();
  }
  (void)ones;
  float* sM = (float*)smem;
  if (kh == 1) {
    float* dst = sM + qt * 17 * 64 + lane;
    dst[0] = m;
#pragma unroll
    for (int i = 0; i < 16; ++i) dst[(i + 1) * 64] = o[i];
  }
  __syncthreads();
  if (kh == 0) {
    const float* src = sM + qt * 17 * 64 + lane;
    const float m2 = src[0];
    const float mn = fmaxf(m, m2);
    const float a1 = __builtin_amdgcn_exp2f(m - mn), a2 = __builtin_amdgcn_exp2f(m2 - mn);
#pragma unroll
    for (int i = 0; i < 16; ++i) o[i] = o[i] * a1 + src[(i + 1) * 64] * a2;
    float l = o[12];
    {
      auto rr = __builtin_amdgcn_permlane32_swap(__float_as_uint(l), __float_as_uint(l), false, false);
      l = (h == 0) ? __uint_as_float(rr[1]) : l;
    }
    const float inv = 1.f / l;
    u16* dst = o_out + (size_t)qrow * 448 + head * 28;
#pragma unroll
    for (int g = 0; g < 4; ++g) {
      const int d0 = 8 * g + 4 * h;
      if (d0 < 28) {
        bf4 v;
#pragma unroll
        for (int j = 0; j < 4; ++j) v[j] = (__bf16)(o[4 * g + j] * inv);
        *(bf4*)(dst + d0) = v;
      }
    }
  }
}

template <typename TX>
__global__ __launch_bounds__(256) void k_oproj(const u16* __restrict__ A, const u16* __restrict__ w, const TX* __restrict__ xin,
                                               float* __restrict__ xout) {
  extern __shared__ __attribute__((aligned(16))) char smem[];
  constexpr int BSTR = 192;
  char* sA = smem;
  char* sB = smem + 64 * ASTR;
  const int tid = threadIdx.x, wid = tid >> 6, lane = tid & 63;
  const int mt = blockIdx.x / 7, nt = blockIdx.x % 7;
  const int r32 = lane & 31, h = lane >> 5;
  const int wm = wid & 1, wn = wid >> 1;
#pragma unroll
  for (int i = 0; i < 14; ++i) {
    int p = tid + 256 * i; int row = p / 56, c = p % 56;
    *(u32x4*)(sA + row * ASTR + c * 16) = *(const u32x4*)(A + (size_t)(mt * 64 + row) * 448 + c * 8);
  }
#pragma unroll
  for (int i = 0; i < 14; ++i) {
    int p = tid + 256 * i; int row = p >> 3, c = p & 7;
    *(u32x4*)(sB + row * BSTR + c * 16) = *(const u32x4*)(w + (size_t)row * 448 + nt * 64 + c * 8);
  }
  __syncthreads();
  f32x16 acc;
#pragma unroll
  for (int i = 0; i < 16; ++i) acc[i] = 0.f;
  const int i16 = lane & 15, q = i16 >> 2, p4 = i16 & 3, blk = (lane >> 4) & 1;
  const int boff = (8 * h + q) * BSTR + (wn * 32 + 16 * blk + 4 * p4) * 2;
  const int aoff = (wm * 32 + r32) * ASTR + 16 * h;
#pragma unroll 4
  for (int ks = 0; ks < 28; ++ks) {
    bf16x8 a = *(const bf16x8*)(sA + aoff + ks * 32);
    s16x4 lo = tr_read(sB + boff + ks * 16 * BSTR);
    s16x4 hi = tr_read(sB + boff + ks * 16 * BSTR + 4 * BSTR);
    acc = MFMA32(a, cat(lo, hi), acc);
  }
  const int n = nt * 64 + wn * 32 + r32;
#pragma unroll
  for (int i = 0; i < 16; ++i) {
    const int row = mt * 64 + wm * 32 + crow(i, h);
    float xv;
    if constexpr (sizeof(TX) == 2) xv = bf2f(((const u16*)xin)[(size_t)row * 448 + n]);
    else xv = ((const float*)xin)[(size_t)row * 448 + n];
    xout[(size_t)row * 448 + n] = xv + acc[i];
  }
}

__global__ __launch_bounds__(256) void k_ln_glu(const float* __restrict__ x, const u16* __restrict__ wg, const u16* __restrict__ wu,
                                                u16* __restrict__ hout) {
  extern __shared__ __attribute__((aligned(16))) char smem[];
  constexpr int BSTR = 576;
  char* sA = smem;
  char* sB = smem + 128 * ASTR;
  const int tid = threadIdx.x, wid = tid >> 6, lane = tid & 63;
  const int mt = blockIdx.x / 14, ft = blockIdx.x % 14;
  const int r32 = lane & 31, h = lane >> 5;
  const int wm = wid & 1, wn = wid >> 1;
  u32x4 st[4];
  auto gload = [&](int kc) {
#pragma unroll
    for (int i = 0; i < 4; ++i) {
      int p = tid + 256 * i; int row = p >> 5, c = p & 31;
      const u16* src = (c < 16) ? wg : wu;
      st[i] = *(const u32x4*)(src + (size_t)(kc * 32 + row) * 1792 + ft * 128 + (c & 15) * 8);
    }
  };
  auto swrite = [&](int buf) {
#pragma unroll
    for (int i = 0; i < 4; ++i) {
      int p = tid + 256 * i; int row = p >> 5, c = p & 31;
      *(u32x4*)(sB + buf * (32 * BSTR) + row * BSTR + c * 16) = st[i];
    }
  };
  gload(0);
  ln_rows_to_lds<float, 128, 4>(x, mt * 128, sA, wid, lane);
  swrite(0);
  __syncthreads();
  f32x16 acc[2][4];
#pragma unroll
  for (int a = 0; a < 2; ++a)
#pragma unroll
    for (int b = 0; b < 4; ++b)
#pragma unroll
      for (int i = 0; i < 16; ++i) acc[a][b][i] = 0.f;
  const int i16 = lane & 15, q = i16 >> 2, p4 = i16 & 3, blk = (lane >> 4) & 1;
  const int boff = (8 * h + q) * BSTR + (wn * 64 + 16 * blk + 4 * p4) * 2;
  const int aoff = (wm * 64 + r32) * ASTR + 16 * h;
  for (int kc = 0; kc < 14; ++kc) {
    if (kc < 13) gload(kc + 1);
    const char* bb = sB + (kc & 1) * (32 * BSTR) + boff;
#pragma unroll
    for (int ks = 0; ks < 2; ++ks) {
      bf16x8 a0 = *(const bf16x8*)(sA + aoff + (kc * 32 + ks * 16) * 2);
      bf16x8 a1 = *(const bf16x8*)(sA + aoff + 32 * ASTR + (kc * 32 + ks * 16) * 2);
#pragma unroll
      for (int t = 0; t < 4; ++t) {
        const int co = (t & 1) * 64 + (t >> 1) * 256;
        s16x4 lo = tr_read(bb + ks * 16 * BSTR + co);
        s16x4 hi = tr_read(bb + ks * 16 * BSTR + 4 * BSTR + co);
        bf16x8 b = cat(lo, hi);
        acc[0][t] = MFMA32(a0, b, acc[0][t]);
        acc[1][t] = MFMA32(a1, b, acc[1][t]);
      }
    }
    if (kc < 13) swrite((kc + 1) & 1);
    __syncthreads();
  }
  constexpr int CSTR = 272;
  char* sC = smem;
#pragma unroll
  for (int a = 0; a < 2; ++a)
#pragma unroll
    for (int t = 0; t < 2; ++t) {
      const int col = wn * 64 + t * 32 + r32;
#pragma unroll
      for (int i = 0; i < 16; ++i) {
        const int row = wm * 64 + a * 32 + crow(i, h);
        const float g = acc[a][t][i], u = acc[a][2 + t][i];
        const float sg = 1.f / (1.f + __expf(-g));
        *(u16*)(sC + row * CSTR + col * 2) = f2bf(sg * u);
      }
    }
  __syncthreads();
#pragma unroll
  for (int i = 0; i < 8; ++i) {
    const int id = tid + 256 * i; const int row = id >> 4, c = id & 15;
    u32x4 v = *(const u32x4*)(sC + row * CSTR + c * 16);
    *(u32x4*)(hout + (size_t)(mt * 128 + row) * 1792 + ft * 128 + c * 8) = v;
  }
}

template <typename TO>
__global__ __launch_bounds__(256) void k_down(const u16* __restrict__ hin, const u16* __restrict__ w, const float* __restrict__ xin,
                                              TO* __restrict__ xout) {
  extern __shared__ __attribute__((aligned(16))) char smem[];
  constexpr int A2STR = 272;
  constexpr int BSTR = 192;
  constexpr int ABUF = 64 * A2STR, BBUF = 128 * BSTR;
  char* sA = smem;
  char* sB = smem + 2 * ABUF;
  const int tid = threadIdx.x, wid = tid >> 6, lane = tid & 63;
  const int mt = blockIdx.x / 7, nt = blockIdx.x % 7;
  const int r32 = lane & 31, h = lane >> 5;
  u32x4 sta[4], stb[4];
  auto gload = [&](int kc) {
#pragma unroll
    for (int i = 0; i < 4; ++i) {
      int p = tid + 256 * i; int row = p >> 4, c = p & 15;
      sta[i] = *(const u32x4*)(hin + (size_t)(mt * 64 + row) * 1792 + kc * 128 + c * 8);
    }
#pragma unroll
    for (int i = 0; i < 4; ++i) {
      int p = tid + 256 * i; int row = p >> 3, c = p & 7;
      stb[i] = *(const u32x4*)(w + (size_t)(kc * 128 + row) * 448 + nt * 64 + c * 8);
    }
  };
  auto swrite = [&](int buf) {
#pragma unroll
    for (int i = 0; i < 4; ++i) {
      int p = tid + 256 * i; int row = p >> 4, c = p & 15;
      *(u32x4*)(sA + buf * ABUF + row * A2STR + c * 16) = sta[i];
    }
#pragma unroll
    for (int i = 0; i < 4; ++i) {
      int p = tid + 256 * i; int row = p >> 3, c = p & 7;
      *(u32x4*)(sB + buf * BBUF + row * BSTR + c * 16) = stb[i];
    }
  };
  gload(0); swrite(0);
  __syncthreads();
  f32x16 acc[2][2];
#pragma unroll
  for (int a = 0; a < 2; ++a)
#pragma unroll
    for (int b = 0; b < 2; ++b)
#pragma unroll
      for (int i = 0; i < 16; ++i) acc[a][b][i] = 0.f;
  const int i16 = lane & 15, q = i16 >> 2, p4 = i16 & 3, blk = (lane >> 4) & 1;
  const int boff = (wid * 32 + 8 * h + q) * BSTR + (16 * blk + 4 * p4) * 2;
  const int aoff = r32 * A2STR + wid * 64 + 16 * h;
  for (int kc = 0; kc < 14; ++kc) {
    if (kc < 13) gload(kc + 1);
    const char* ab = sA + (kc & 1) * ABUF + aoff;
    const char* bb = sB + (kc & 1) * BBUF + boff;
#pragma unroll
    for (int ks = 0; ks < 2; ++ks) {
      bf16x8 a0 = *(const bf16x8*)(ab + ks * 32);
      bf16x8 a1 = *(const bf16x8*)(ab + 32 * A2STR + ks * 32);
#pragma unroll
      for (int t = 0; t < 2; ++t) {
        s16x4 lo = tr_read(bb + ks * 16 * BSTR + t * 64);
        s16x4 hi = tr_read(bb + ks * 16 * BSTR + 4 * BSTR + t * 64);
        bf16x8 b = cat(lo, hi);
        acc[0][t] = MFMA32(a0, b, acc[0][t]);
        acc[1][t] = MFMA32(a1, b, acc[1][t]);
      }
    }
    if (kc < 13) swrite((kc + 1) & 1);
    __syncthreads();
  }
  float* sR = (float*)smem;
#pragma unroll
  for (int a = 0; a < 2; ++a)
#pragma unroll
    for (int t = 0; t < 2; ++t)
#pragma unroll
      for (int i = 0; i < 16; ++i) {
        const int row = a * 32 + crow(i, h), col = t * 32 + r32;
        sR[wid * 4096 + row * 64 + col] = acc[a][t][i];
      }
  __syncthreads();
#pragma unroll
  for (int i = 0; i < 4; ++i) {
    const int id = tid + 256 * i; const int row = id >> 4, c4 = (id & 15) * 4;
    f32x4 v = *(const f32x4*)(sR + row * 64 + c4);
#pragma unroll
    for (int w2 = 1; w2 < 4; ++w2) v += *(const f32x4*)(sR + w2 * 4096 + row * 64 + c4);
    const size_t gi = (size_t)(mt * 64 + row) * 448 + nt * 64 + c4;
    f32x4 xi = *(const f32x4*)(xin + gi);
    v += xi;
    if constexpr (sizeof(TO) == 4) *(f32x4*)((float*)xout + gi) = v;
    else {
      bf4 b;
#pragma unroll
      for (int j = 0; j < 4; ++j) b[j] = (__bf16)v[j];
      *(bf4*)((u16*)xout + gi) = b;
    }
  }
}

extern "C" void kernel_launch(void* const* d_in, const int* in_sizes, int n_in, void* d_out, int out_size, void* d_ws,
                              size_t ws_size, hipStream_t stream) {
  const u16* Ax = (const u16*)d_in[0];
  const u16* wqkv = (const u16*)d_in[1];
  const u16* wo = (const u16*)d_in[2];
  const u16* wg = (const u16*)d_in[4];
  const u16* wu = (const u16*)d_in[5];
  const u16* wd = (const u16*)d_in[6];
  char* ws = (char*)d_ws;
  float* xa = (float*)ws;
  float* xb = (float*)(ws + 4 * 1024 * 1024);
  u16* qkvp = (u16*)(ws + 8 * 1024 * 1024);
  u16* obuf = (u16*)(ws + 16 * 1024 * 1024);
  u16* hbuf = (u16*)(ws + 20 * 1024 * 1024);
  const size_t smem1 = 64 * ASTR + 2 * 64 * 448;
  const size_t smem2 = 32768;
  const size_t smem3 = 64 * ASTR + 448 * 192;
  const size_t smem4 = 128 * ASTR + 2 * 32 * 576;
  const size_t smem5 = 2 * 64 * 272 + 2 * 128 * 192;
  for (int layer = 0; layer < 2; ++layer) {
    if (layer == 0) k_ln_qkv<u16><<<224, 256, smem1, stream>>>(Ax, wqkv, qkvp);
    else k_ln_qkv<float><<<224, 256, smem1, stream>>>(xa, wqkv, qkvp);
    k_attn<<<256, 512, smem2, stream>>>(qkvp, obuf);
    if (layer == 0) k_oproj<u16><<<224, 256, smem3, stream>>>(obuf, wo, Ax, xb);
    else k_oproj<float><<<224, 256, smem3, stream>>>(obuf, wo, xa, xb);
    k_ln_glu<<<224, 256, smem4, stream>>>(xb, wg, wu, hbuf);
    if (layer == 0) k_down<float><<<224, 256, smem5, stream>>>(hbuf, wd, xb, xa);
    else k_down<u16><<<224, 256, smem5, stream>>>(hbuf, wd, xb, (u16*)d_out);
  }
}
```

```cpp
#include <hip/hip_runtime.h>
#include <hip/hip_bf16.h>
#include <stdint.h>

constexpr int S_ = 2048, D_ = 448, H_ = 16, DH_ = 28, F_ = 1792;

typedef unsigned short u16;
typedef __attribute__((ext_vector_type(8))) short bf16x8;
typedef __attribute__((ext_vector_type(4))) short s16x4;
typedef __attribute__((ext_vector_type(16))) float f32x16;
typedef __attribute__((ext_vector_type(8))) float f32x8;
typedef __attribute__((ext_vector_type(4))) float f32x4;
typedef __attribute__((ext_vector_type(4))) unsigned u32x4;
typedef __attribute__((ext_vector_type(2))) unsigned u32x2;
typedef __attribute__((ext_vector_type(8))) __bf16 bf8;
typedef __attribute__((ext_vector_type(4))) __bf16 bf4;
typedef __attribute__((ext_vector_type(2))) __bf16 bf2;
typedef __attribute__((address_space(3))) s16x4* lds_s16x4_p;

#define DI __device__ __forceinline__
#define MFMA32(a, b, c) __builtin_amdgcn_mfma_f32_32x32x16_bf16((a), (b), (c), 0, 0, 0)

DI int crow(int reg, int h) { return (reg & 3) + 8 * (reg >> 2) + 4 * h; }

DI s16x4 tr_read(const char* p) {
#if defined(__HIP_DEVICE_COMPILE__)
  return __builtin_amdgcn_ds_read_tr16_b64_v4i16((lds_s16x4_p)(p));
#else
  return s16x4{};
#endif
}
DI bf16x8 cat(s16x4 lo, s16x4 hi) { return __builtin_shufflevector(lo, hi, 0, 1, 2, 3, 4, 5, 6, 7); }
DI bf16x8 pack8(f32x8 v) { return __builtin_bit_cast(bf16x8, __builtin_convertvector(v, bf8)); }
DI u16 f2bf(float f) { return __builtin_bit_cast(u16, (__bf16)f); }
DI float bf2f(u16 u) { return __uint_as_float(((unsigned)u) << 16); }
DI float wave_sum(float v) {
#pragma unroll
  for (int o = 32; o >= 1; o >>= 1) v += __shfl_xor(v, o, 64);
  return v;
}

DI void load8(const float* p, float* v) {
  f32x4 a = *(const f32x4*)p, b = *(const f32x4*)(p + 4);
  v[0] = a[0]; v[1] = a[1]; v[2] = a[2]; v[3] = a[3]; v[4] = b[0]; v[5] = b[1]; v[6] = b[2]; v[7] = b[3];
}
DI void load8(const u16* p, float* v) {
  u32x4 a = *(const u32x4*)p;
#pragma unroll
  for (int i = 0; i < 4; ++i) { v[2 * i] = __uint_as_float(a[i] << 16); v[2 * i + 1] = __uint_as_float(a[i] & 0xffff0000u); }
}

constexpr int ASTR = 912;

template <typename TX, int NROWS, int NW>
DI void ln_rows_to_lds(const TX* __restrict__ x, int row0, char* sA, int wid, int lane) {
  constexpr int RPW = NROWS / NW;
  constexpr int EPP = 16 / sizeof(TX);
  constexpr int NP = 448 / EPP / 4;
  const int rr = lane >> 2, j4 = lane & 3;
#pragma unroll
  for (int pass = 0; pass < RPW / 16; ++pass) {
    const int r = wid * RPW + pass * 16 + rr;
    const TX* src = x + (size_t)(row0 + r) * D_;
    u32x4 raw[NP];
#pragma unroll
    for (int i = 0; i < NP; ++i) raw[i] = *(const u32x4*)(src + (j4 + 4 * i) * EPP);
    float v[NP * EPP];
#pragma unroll
    for (int i = 0; i < NP; ++i) {
      if constexpr (sizeof(TX) == 4) {
#pragma unroll
        for (int e = 0; e < 4; ++e) v[i * 4 + e] = __uint_as_float(raw[i][e]);
      } else {
#pragma unroll
        for (int e = 0; e < 4; ++e) { v[i * 8 + 2 * e] = __uint_as_float(raw[i][e] << 16); v[i * 8 + 2 * e + 1] = __uint_as_float(raw[i][e] & 0xffff0000u); }
      }
    }
    float s = 0.f;
#pragma unroll
    for (int j = 0; j < NP * EPP; ++j) s += v[j];
    s += __shfl_xor(s, 1, 64); s += __shfl_xor(s, 2, 64);
    const float mean = s * (1.f / 448.f);
    float q = 0.f;
#pragma unroll
    for (int j = 0; j < NP * EPP; ++j) { float d = v[j] - mean; q += d * d; }
    q += __shfl_xor(q, 1, 64); q += __shfl_xor(q, 2, 64);
    const float rstd = rsqrtf(q * (1.f / 448.f) + 1e-5f);
    char* dst = sA + r * ASTR;
#pragma unroll
    for (int i = 0; i < NP; ++i) {
      if constexpr (sizeof(TX) == 4) {
        bf4 o;
#pragma unroll
        for (int e = 0; e < 4; ++e) o[e] = (__bf16)((v[i * 4 + e] - mean) * rstd);
        *(bf4*)(dst + (j4 + 4 * i) * 8) = o;
      } else {
        f32x8 o;
#pragma unroll
        for (int e = 0; e < 8; ++e) o[e] = (v[i * 8 + e] - mean) * rstd;
        *(bf16x8*)(dst + (j4 + 4 * i) * 16) = pack8(o);
      }
    }
  }
}

template <int NROWS, int NW>
DI void ln_rows_from_stats(const u16* __restrict__ x, const float2* __restrict__ stats, int row0, char* sA, int wid, int lane) {
  constexpr int RPW = NROWS / NW;
  constexpr int NP = 14;
  const int rr = lane >> 2, j4 = lane & 3;
#pragma unroll
  for (int pass = 0; pass < RPW / 16; ++pass) {
    const int r = wid * RPW + pass * 16 + rr;
    const u16* src = x + (size_t)(row0 + r) * D_;
    u32x4 raw[NP];
#pragma unroll
    for (int i = 0; i < NP; ++i) raw[i] = *(const u32x4*)(src + (j4 + 4 * i) * 8);
    const f32x4* sp = (const f32x4*)(stats + (size_t)(row0 + r) * 8);
    f32x4 s01 = sp[0], s23 = sp[1], s45 = sp[2], s6x = sp[3];
    const float sum = s01[0] + s01[2] + s23[0] + s23[2] + s45[0] + s45[2] + s6x[0];
    const float mean = sum * (1.f / 448.f);
    float m2 = s01[1] + s01[3] + s23[1] + s23[3] + s45[1] + s45[3] + s6x[1];
    float dv = 0.f, t;
    t = s01[0] * (1.f / 64.f) - mean; dv += t * t;
    t = s01[2] * (1.f / 64.f) - mean; dv += t * t;
    t = s23[0] * (1.f / 64.f) - mean; dv += t * t;
    t = s23[2] * (1.f / 64.f) - mean; dv += t * t;
    t = s45[0] * (1.f / 64.f) - mean; dv += t * t;
    t = s45[2] * (1.f / 64.f) - mean; dv += t * t;
    t = s6x[0] * (1.f / 64.f) - mean; dv += t * t;
    m2 += 64.f * dv;
    const float rstd = rsqrtf(m2 * (1.f / 448.f) + 1e-5f);
    const float nb = -mean * rstd;
    char* dst = sA + r * ASTR;
#pragma unroll
    for (int i = 0; i < NP; ++i) {
      f32x8 o;
#pragma unroll
      for (int e = 0; e < 4; ++e) {
        o[2 * e] = __uint_as_float(raw[i][e] << 16) * rstd + nb;
        o[2 * e + 1] = __uint_as_float(raw[i][e] & 0xffff0000u) * rstd + nb;
      }
      *(bf16x8*)(dst + (j4 + 4 * i) * 16) = pack8(o);
    }
  }
}

DI void write_partial_stats(f32x4 v, float2* __restrict__ stats, int grow, int nt, int c16) {
  float s = v[0] + v[1] + v[2] + v[3];
  s += __shfl_xor(s, 1, 64); s += __shfl_xor(s, 2, 64); s += __shfl_xor(s, 4, 64); s += __shfl_xor(s, 8, 64);
  const float mp = s * (1.f / 64.f);
  float q = (v[0] - mp) * (v[0] - mp) + (v[1] - mp) * (v[1] - mp) + (v[2] - mp) * (v[2] - mp) + (v[3] - mp) * (v[3] - mp);
  q += __shfl_xor(q, 1, 64); q += __shfl_xor(q, 2, 64); q += __shfl_xor(q, 4, 64); q += __shfl_xor(q, 8, 64);
  if (c16 == 0) stats[(size_t)grow * 8 + nt] = make_float2(s, q);
}

template <bool FIRST>
__global__ __launch_bounds__(768) void k_ln_qkv(const u16* __restrict__ x, const u16* __restrict__ w, u16* __restrict__ qkvp, const float2* __restrict__ stats) {
  extern __shared__ __attribute__((aligned(16))) char smem[];
  constexpr int BSTR = 448;
  char* sA = smem;
  char* sB = smem + 64 * ASTR;
  const int tid = threadIdx.x, wid = tid >> 6, lane = tid & 63;
  const int xcd_ = blockIdx.x & 7, idx_ = blockIdx.x >> 3;
  const int mt = xcd_ * 4 + idx_ / 7, nt = idx_ % 7;
  const int r32 = lane & 31, h = lane >> 5;
  const int wm = wid & 1, wn = wid >> 1;

  u32x4 st[2][2];
  auto gload = [&](int kc, u32x4* stp) {
#pragma unroll
    for (int i = 0; i < 2; ++i) {
      int p = tid + 768 * i; int row = p / 24, c = p % 24;
      stp[i] = *(const u32x4*)(w + (size_t)(kc * 64 + row) * 1344 + nt * 192 + c * 8);
    }
  };
  auto swrite = [&](int buf, const u32x4* stp) {
#pragma unroll
    for (int i = 0; i < 2; ++i) {
      int p = tid + 768 * i; int row = p / 24, c = p % 24;
      *(u32x4*)(sB + buf * (64 * BSTR) + row * BSTR + c * 16) = stp[i];
    }
  };
  gload(0, st[0]); gload(1, st[1]);
  if (wid < 4) {
    if constexpr (FIRST) ln_rows_to_lds<u16, 64, 4>(x, mt * 64, sA, wid, lane);
    else ln_rows_from_stats<64, 4>(x, stats, mt * 64, sA, wid, lane);
  }
  swrite(0, st[0]);
  __syncthreads();

  f32x16 acc;
#pragma unroll
  for (int i = 0; i < 16; ++i) acc[i] = 0.f;

  const int i16 = lane & 15, q = i16 >> 2, p4 = i16 & 3, blk = (lane >> 4) & 1;
  const int boff = (8 * h + q) * BSTR + (wn * 32 + 16 * blk + 4 * p4) * 2;
  const int aoff = (wm * 32 + r32) * ASTR + 16 * h;
#pragma unroll
  for (int kc = 0; kc < 7; ++kc) {
    if (kc + 2 < 7) gload(kc + 2, st[kc & 1]);
    const char* bb = sB + (kc & 1) * (64 * BSTR) + boff;
#pragma unroll
    for (int ks = 0; ks < 4; ++ks) {
      bf16x8 a = *(const bf16x8*)(sA + aoff + (kc * 64 + ks * 16) * 2);
      s16x4 lo = tr_read(bb + ks * 16 * BSTR);
      s16x4 hi = tr_read(bb + ks * 16 * BSTR + 4 * BSTR);
      acc = MFMA32(a, cat(lo, hi), acc);
    }
    if (kc + 1 < 7) swrite((kc + 1) & 1, st[(kc + 1) & 1]);
    __syncthreads();
  }
  constexpr int CSTR = 400;
  char* sC = smem;
  const float SC = 0.18898223650461362f * 1.4426950408889634f;
  {
    const int col = wn * 32 + r32;
    const float sc = (nt * 192 + col) < 448 ? SC : 1.f;
#pragma unroll
    for (int i = 0; i < 16; ++i) {
      const int row = wm * 32 + crow(i, h);
      *(u16*)(sC + row * CSTR + col * 2) = f2bf(acc[i] * sc);
    }
  }
  __syncthreads();
#pragma unroll
  for (int i = 0; i < 4; ++i) {
    const int id = tid + 768 * i; const int row = id / 48, pc = id % 48;
    const int n = nt * 192 + pc * 4;
    const int sec = n / 448, rem = n % 448, head = rem / 28, d0 = rem % 28;
    u32x2 v = *(const u32x2*)(sC + row * CSTR + pc * 8);
    u16* dst = qkvp + ((size_t)(sec * 16 + head) * 2048 + (mt * 64 + row)) * 32 + d0;
    *(u32x2*)dst = v;
    if (d0 == 24) {
      u32x2 pad; pad[0] = (sec == 2) ? 0x00003F80u : 0u; pad[1] = 0u;
      *(u32x2*)(dst + 4) = pad;
    }
  }
}

constexpr float RTHR = 8.f;
DI float max3f(float a, float b, float c) { return __builtin_elementwise_maximum(__builtin_elementwise_maximum(a, b), c); }
__global__ __launch_bounds__(1024) void k_attn(const u16* __restrict__ qkvp, u16* __restrict__ o_out) {
  extern __shared__ __attribute__((aligned(16))) char smem[];
  const int tid = threadIdx.x, wid = tid >> 6, lane = tid & 63;
  const int xcd_ = blockIdx.x & 7, idx_ = blockIdx.x >> 3;
  const int head = xcd_ * 2 + (idx_ >> 4), qb = idx_ & 15;
  const int qt = wid & 3, kq = wid >> 2;
  const int r32 = lane & 31, h = lane >> 5;
  const u16* Q = qkvp + (size_t)(head) * 2048 * 32;
  const u16* K = qkvp + (size_t)(16 + head) * 2048 * 32;
  const u16* V = qkvp + (size_t)(32 + head) * 2048 * 32;
  const int qrow = qb * 128 + qt * 32 + r32;
  const bf16x8 q0 = *(const bf16x8*)(Q + (size_t)qrow * 32 + 8 * h);
  const bf16x8 q1 = *(const bf16x8*)(Q + (size_t)qrow * 32 + 16 + 8 * h);
  const int squart = tid >> 8, st = tid & 255, skey = st >> 2, sslot = st & 3;
  const u16* gK = K + (size_t)(squart * 512 + skey) * 32 + sslot * 8;
  const u16* gV = V + (size_t)(squart * 512 + skey) * 32 + sslot * 8;
  char* sbase_st = smem + squart * 16384;
  const int kst_off = skey * 64 + ((sslot ^ ((skey >> 2) & 3)) * 16);
  const int vst_off = 4096 + skey * 64 + sslot * 16;
  u32x4 rg[2][2];
  auto gload = [&](int it, u32x4* r) {
    r[0] = *(const u32x4*)(gK + (size_t)it * 64 * 32);
    r[1] = *(const u32x4*)(gV + (size_t)it * 64 * 32);
  };
  auto swrite = [&](int buf, const u32x4* r) {
    char* sw = sbase_st + buf * 8192;
    *(u32x4*)(sw + kst_off) = r[0];
    *(u32x4*)(sw + vst_off) = r[1];
  };
  gload(0, rg[0]); gload(1, rg[1]);
  swrite(0, rg[0]);
  __syncthreads();

  const char* sbase = smem + kq * 16384;
  const int swz = (r32 >> 2) & 3;
  const int ka_off0 = r32 * 64 + ((h ^ swz) * 16);
  const int ka_off1 = r32 * 64 + (((2 + h) ^ swz) * 16);
  const int i16 = lane & 15, q4 = i16 >> 2, p4 = i16 & 3, blk = (lane >> 4) & 1;
  const int v_off = 4096 + (4 * h + q4) * 64 + (16 * blk + 4 * p4) * 2;

  f32x16 o, cneg;
#pragma unroll
  for (int i = 0; i < 16; ++i) { o[i] = 0.f; cneg[i] = 0.f; }
  float m = 0.f;
#pragma unroll
  for (int it = 0; it < 8; ++it) {
    if (it + 2 < 8) gload(it + 2, rg[it & 1]);
    {
      const char* sb = sbase + (it & 1) * 8192;
      bf16x8 ka0 = *(const bf16x8*)(sb + ka_off0);
      bf16x8 ka1 = *(const bf16x8*)(sb + ka_off1);
      bf16x8 kb0 = *(const bf16x8*)(sb + 2048 + ka_off0);
      bf16x8 kb1 = *(const bf16x8*)(sb + 2048 + ka_off1);
      f32x16 s0 = MFMA32(ka0, q0, cneg); s0 = MFMA32(ka1, q1, s0);
      f32x16 s1 = MFMA32(kb0, q0, cneg); s1 = MFMA32(kb1, q1, s1);
      float mx = __builtin_elementwise_maximum(s0[0], s1[0]);
#pragma unroll
      for (int i = 1; i < 16; ++i) mx = max3f(mx, s0[i], s1[i]);
      {
        auto rr = __builtin_amdgcn_permlane32_swap(__float_as_uint(mx), __float_as_uint(mx), false, false);
        mx = __builtin_elementwise_maximum(__uint_as_float(rr[0]), __uint_as_float(rr[1]));
      }
      const bool first = (it == 0);
      if (first || __any(mx > RTHR)) {
        const float d = first ? mx : __builtin_elementwise_maximum(mx, 0.f);
        const float alpha = first ? 1.f : __builtin_amdgcn_exp2f(-d);
#pragma unroll
        for (int i = 0; i < 16; ++i) { o[i] *= alpha; s0[i] -= d; s1[i] -= d; }
        m += d;
#pragma unroll
        for (int i = 0; i < 16; ++i) cneg[i] = -m;
      }
      f32x8 e0, e1, e2, e3;
#pragma unroll
      for (int i = 0; i < 8; ++i) {
        e0[i] = __builtin_amdgcn_exp2f(s0[i]); e1[i] = __builtin_amdgcn_exp2f(s0[8 + i]);
        e2[i] = __builtin_amdgcn_exp2f(s1[i]); e3[i] = __builtin_amdgcn_exp2f(s1[8 + i]);
      }
      bf16x8 p0 = pack8(e0), p1 = pack8(e1), p2 = pack8(e2), p3 = pack8(e3);
      const char* vb = sb + v_off;
      o = MFMA32(cat(tr_read(vb), tr_read(vb + 8 * 64)), p0, o);
      o = MFMA32(cat(tr_read(vb + 16 * 64), tr_read(vb + 24 * 64)), p1, o);
      o = MFMA32(cat(tr_read(vb + 32 * 64), tr_read(vb + 40 * 64)), p2, o);
      o = MFMA32(cat(tr_read(vb + 48 * 64), tr_read(vb + 56 * 64)), p3, o);
    }
    if (it + 1 < 8) swrite((it + 1) & 1, rg[(it + 1) & 1]);
    __syncthreads();
  }
  float* sM = (float*)smem;
  if (kq != 0) {
    float* dst = sM + ((kq - 1) * 4 + qt) * 17 * 64 + lane;
    dst[0] = m;
#pragma unroll
    for (int i = 0; i < 16; ++i) dst[(i + 1) * 64] = o[i];
  }
  __syncthreads();
  if (kq == 0) {
#pragma unroll
    for (int j = 0; j < 3; ++j) {
      const float* src = sM + (j * 4 + qt) * 17 * 64 + lane;
      const float m2 = src[0];
      const float mn = __builtin_elementwise_maximum(m, m2);
      const float a1 = __builtin_amdgcn_exp2f(m - mn), a2 = __builtin_amdgcn_exp2f(m2 - mn);
#pragma unroll
      for (int i = 0; i < 16; ++i) o[i] = o[i] * a1 + src[(i + 1) * 64] * a2;
      m = mn;
    }
    float l = o[12];
    {
      auto rr = __builtin_amdgcn_permlane32_swap(__float_as_uint(l), __float_as_uint(l), false, false);
      l = (h == 0) ? __uint_as_float(rr[1]) : l;
    }
    const float inv = 1.f / l;
    u16* dst = o_out + (size_t)qrow * 448 + head * 28;
#pragma unroll
    for (int g = 0; g < 4; ++g) {
      const int d0 = 8 * g + 4 * h;
      if (d0 < 28) {
        bf4 v;
#pragma unroll
        for (int j = 0; j < 4; ++j) v[j] = (__bf16)(o[4 * g + j] * inv);
        *(bf4*)(dst + d0) = v;
      }
    }
  }
}

__global__ __launch_bounds__(256) void k_oproj(const u16* __restrict__ A, const u16* __restrict__ w, const u16* __restrict__ xin,
                                               u16* __restrict__ xout, float2* __restrict__ stats) {
  extern __shared__ __attribute__((aligned(16))) char smem[];
  constexpr int BSTR = 192;
  char* sA = smem;
  char* sB = smem + 64 * ASTR;
  const int tid = threadIdx.x, wid = tid >> 6, lane = tid & 63;
  const int xcd_ = blockIdx.x & 7, idx_ = blockIdx.x >> 3;
  const int mt = xcd_ * 4 + idx_ / 7, nt = idx_ % 7;
  const int r32 = lane & 31, h = lane >> 5;
  const int wm = wid & 1, wn = wid >> 1;
  {
    u32x4 ra[14], rb[14];
#pragma unroll
    for (int i = 0; i < 14; ++i) {
      int p = tid + 256 * i; int row = p / 56, c = p % 56;
      ra[i] = *(const u32x4*)(A + (size_t)(mt * 64 + row) * 448 + c * 8);
    }
#pragma unroll
    for (int i = 0; i < 14; ++i) {
      int p = tid + 256 * i; int row = p >> 3, c = p & 7;
      rb[i] = *(const u32x4*)(w + (size_t)row * 448 + nt * 64 + c * 8);
    }
#pragma unroll
    for (int i = 0; i < 14; ++i) {
      int p = tid + 256 * i; int row = p / 56, c = p % 56;
      *(u32x4*)(sA + row * ASTR + c * 16) = ra[i];
    }
#pragma unroll
    for (int i = 0; i < 14; ++i) {
      int p = tid + 256 * i; int row = p >> 3, c = p & 7;
      *(u32x4*)(sB + row * BSTR + c * 16) = rb[i];
    }
  }
  __syncthreads();
  f32x16 acc;
#pragma unroll
  for (int i = 0; i < 16; ++i) acc[i] = 0.f;
  const int i16 = lane & 15, q = i16 >> 2, p4 = i16 & 3, blk = (lane >> 4) & 1;
  const int boff = (8 * h + q) * BSTR + (wn * 32 + 16 * blk + 4 * p4) * 2;
  const int aoff = (wm * 32 + r32) * ASTR + 16 * h;
#pragma unroll 4
  for (int ks = 0; ks < 28; ++ks) {
    bf16x8 a = *(const bf16x8*)(sA + aoff + ks * 32);
    s16x4 lo = tr_read(sB + boff + ks * 16 * BSTR);
    s16x4 hi = tr_read(sB + boff + ks * 16 * BSTR + 4 * BSTR);
    acc = MFMA32(a, cat(lo, hi), acc);
  }
  __syncthreads();
  float* sR = (float*)smem;
#pragma unroll
  for (int i = 0; i < 16; ++i) sR[(wm * 32 + crow(i, h)) * 64 + wn * 32 + r32] = acc[i];
  __syncthreads();
#pragma unroll
  for (int i = 0; i < 4; ++i) {
    const int id = tid + 256 * i; const int row = id >> 4, c16 = id & 15, c4 = c16 * 4;
    const size_t gi = (size_t)(mt * 64 + row) * 448 + nt * 64 + c4;
    f32x4 xi;
    {
      u32x2 r2 = *(const u32x2*)(xin + gi);
      xi[0] = __uint_as_float(r2[0] << 16); xi[1] = __uint_as_float(r2[0] & 0xffff0000u);
      xi[2] = __uint_as_float(r2[1] << 16); xi[3] = __uint_as_float(r2[1] & 0xffff0000u);
    }
    f32x4 v = *(const f32x4*)(sR + row * 64 + c4) + xi;
    bf4 b;
#pragma unroll
    for (int j = 0; j < 4; ++j) { b[j] = (__bf16)v[j]; v[j] = (float)b[j]; }
    *(bf4*)(xout + gi) = b;
    write_partial_stats(v, stats, mt * 64 + row, nt, c16);
  }
}

__global__ __launch_bounds__(512) void k_ln_glu(const u16* __restrict__ x, const u16* __restrict__ wg, const u16* __restrict__ wu,
                                                u16* __restrict__ hout, const float2* __restrict__ stats) {
  extern __shared__ __attribute__((aligned(16))) char smem[];
  constexpr int BSTR = 576;
  char* sA = smem;
  char* sB = smem + 128 * ASTR;
  const int tid = threadIdx.x, wid = tid >> 6, lane = tid & 63;
  const int xcd_ = blockIdx.x & 7, idx_ = blockIdx.x >> 3;
  const int mt = (xcd_ >> 1) * 4 + idx_ / 7, ft = (xcd_ & 1) * 7 + idx_ % 7;
  const int r32 = lane & 31, h = lane >> 5;
  const int wm = wid & 3, wn = wid >> 2;
  u32x4 st[2][2];
  auto gload = [&](int kc, u32x4* stp) {
#pragma unroll
    for (int i = 0; i < 2; ++i) {
      int p = tid + 512 * i; int row = p >> 5, c = p & 31;
      const u16* src = (c < 16) ? wg : wu;
      stp[i] = *(const u32x4*)(src + (size_t)(kc * 32 + row) * 1792 + ft * 128 + (c & 15) * 8);
    }
  };
  auto swrite = [&](int buf, const u32x4* stp) {
#pragma unroll
    for (int i = 0; i < 2; ++i) {
      int p = tid + 512 * i; int row = p >> 5, c = p & 31;
      *(u32x4*)(sB + buf * (32 * BSTR) + row * BSTR + c * 16) = stp[i];
    }
  };
  gload(0, st[0]); gload(1, st[1]);
  ln_rows_from_stats<128, 8>(x, stats, mt * 128, sA, wid, lane);
  swrite(0, st[0]);
  __syncthreads();
  f32x16 acc[4];
#pragma unroll
  for (int b = 0; b < 4; ++b)
#pragma unroll
    for (int i = 0; i < 16; ++i) acc[b][i] = 0.f;
  const int i16 = lane & 15, q = i16 >> 2, p4 = i16 & 3, blk = (lane >> 4) & 1;
  const int boff = (8 * h + q) * BSTR + (wn * 64 + 16 * blk + 4 * p4) * 2;
  const int aoff = (wm * 32 + r32) * ASTR + 16 * h;
#pragma unroll
  for (int kc = 0; kc < 14; ++kc) {
    if (kc + 2 < 14) gload(kc + 2, st[kc & 1]);
    const char* bb = sB + (kc & 1) * (32 * BSTR) + boff;
#pragma unroll
    for (int ks = 0; ks < 2; ++ks) {
      bf16x8 a0 = *(const bf16x8*)(sA + aoff + (kc * 32 + ks * 16) * 2);
#pragma unroll
      for (int t = 0; t < 4; ++t) {
        const int co = (t & 1) * 64 + (t >> 1) * 256;
        s16x4 lo = tr_read(bb + ks * 16 * BSTR + co);
        s16x4 hi = tr_read(bb + ks * 16 * BSTR + 4 * BSTR + co);
        acc[t] = MFMA32(a0, cat(lo, hi), acc[t]);
      }
    }
    if (kc + 1 < 14) swrite((kc + 1) & 1, st[(kc + 1) & 1]);
    __syncthreads();
  }
  constexpr int CSTR = 272;
  char* sC = smem;
#pragma unroll
  for (int t = 0; t < 2; ++t) {
    const int col = wn * 64 + t * 32 + r32;
#pragma unroll
    for (int i = 0; i < 16; ++i) {
      const int row = wm * 32 + crow(i, h);
      const float g = acc[t][i], u = acc[2 + t][i];
      const float sg = 1.f / (1.f + __expf(-g));
      *(u16*)(sC + row * CSTR + col * 2) = f2bf(sg * u);
    }
  }
  __syncthreads();
#pragma unroll
  for (int i = 0; i < 4; ++i) {
    const int id = tid + 512 * i; const int row = id >> 4, c = id & 15;
    u32x4 v = *(const u32x4*)(sC + row * CSTR + c * 16);
    *(u32x4*)(hout + (size_t)(mt * 128 + row) * 1792 + ft * 128 + c * 8) = v;
  }
}

__global__ __launch_bounds__(512) void k_down(const u16* __restrict__ hin, const u16* __restrict__ w, const u16* __restrict__ xin,
                                              u16* __restrict__ xout, float2* __restrict__ stats) {
  extern __shared__ __attribute__((aligned(16))) char smem[];
  constexpr int A2STR = 272;
  constexpr int BSTR = 192;
  constexpr int ABUF = 64 * A2STR, BBUF = 128 * BSTR;
  char* sA = smem;
  char* sB = smem + 2 * ABUF;
  const int tid = threadIdx.x, wid = tid >> 6, lane = tid & 63;
  const int xcd_ = blockIdx.x & 7, idx_ = blockIdx.x >> 3;
  const int mt = xcd_ * 4 + idx_ / 7, nt = idx_ % 7;
  const int r32 = lane & 31, h = lane >> 5;
  u32x4 sta[2][2], stb[2][2];
  auto gload = [&](int kc, u32x4* pa, u32x4* pb) {
#pragma unroll
    for (int i = 0; i < 2; ++i) {
      int p = tid + 512 * i; int row = p >> 4, c = p & 15;
      pa[i] = *(const u32x4*)(hin + (size_t)(mt * 64 + row) * 1792 + kc * 128 + c * 8);
    }
#pragma unroll
    for (int i = 0; i < 2; ++i) {
      int p = tid + 512 * i; int row = p >> 3, c = p & 7;
      pb[i] = *(const u32x4*)(w + (size_t)(kc * 128 + row) * 448 + nt * 64 + c * 8);
    }
  };
  auto swrite = [&](int buf, const u32x4* pa, const u32x4* pb) {
#pragma unroll
    for (int i = 0; i < 2; ++i) {
      int p = tid + 512 * i; int row = p >> 4, c = p & 15;
      *(u32x4*)(sA + buf * ABUF + row * A2STR + c * 16) = pa[i];
    }
#pragma unroll
    for (int i = 0; i < 2; ++i) {
      int p = tid + 512 * i; int row = p >> 3, c = p & 7;
      *(u32x4*)(sB + buf * BBUF + row * BSTR + c * 16) = pb[i];
    }
  };
  gload(0, sta[0], stb[0]); gload(1, sta[1], stb[1]);
  swrite(0, sta[0], stb[0]);
  __syncthreads();
  f32x16 acc[2][2];
#pragma unroll
  for (int a = 0; a < 2; ++a)
#pragma unroll
    for (int b = 0; b < 2; ++b)
#pragma unroll
      for (int i = 0; i < 16; ++i) acc[a][b][i] = 0.f;
  const int i16 = lane & 15, q = i16 >> 2, p4 = i16 & 3, blk = (lane >> 4) & 1;
  const int boff = (wid * 16 + 8 * h + q) * BSTR + (16 * blk + 4 * p4) * 2;
  const int aoff = r32 * A2STR + wid * 32 + 16 * h;
#pragma unroll
  for (int kc = 0; kc < 14; ++kc) {
    if (kc + 2 < 14) gload(kc + 2, sta[kc & 1], stb[kc & 1]);
    const char* ab = sA + (kc & 1) * ABUF + aoff;
    const char* bb = sB + (kc & 1) * BBUF + boff;
    {
      bf16x8 a0 = *(const bf16x8*)(ab);
      bf16x8 a1 = *(const bf16x8*)(ab + 32 * A2STR);
#pragma unroll
      for (int t = 0; t < 2; ++t) {
        s16x4 lo = tr_read(bb + t * 64);
        s16x4 hi = tr_read(bb + 4 * BSTR + t * 64);
        bf16x8 b = cat(lo, hi);
        acc[0][t] = MFMA32(a0, b, acc[0][t]);
        acc[1][t] = MFMA32(a1, b, acc[1][t]);
      }
    }
    if (kc + 1 < 14) swrite((kc + 1) & 1, sta[(kc + 1) & 1], stb[(kc + 1) & 1]);
    __syncthreads();
  }
  float* sR = (float*)smem;
#pragma unroll
  for (int a = 0; a < 2; ++a)
#pragma unroll
    for (int t = 0; t < 2; ++t)
#pragma unroll
      for (int i = 0; i < 16; ++i) {
        const int row = a * 32 + crow(i, h), col = t * 32 + r32;
        sR[wid * 4096 + row * 64 + col] = acc[a][t][i];
      }
  __syncthreads();
#pragma unroll
  for (int i = 0; i < 2; ++i) {
    const int id = tid + 512 * i; const int row = id >> 4, c4 = (id & 15) * 4;
    const size_t gi = (size_t)(mt * 64 + row) * 448 + nt * 64 + c4;
    u32x2 r2 = *(const u32x2*)(xin + gi);
    f32x4 v = *(const f32x4*)(sR + row * 64 + c4);
#pragma unroll
    for (int w2 = 1; w2 < 8; ++w2) v += *(const f32x4*)(sR + w2 * 4096 + row * 64 + c4);
    v[0] += __uint_as_float(r2[0] << 16); v[1] += __uint_as_float(r2[0] & 0xffff0000u);
    v[2] += __uint_as_float(r2[1] << 16); v[3] += __uint_as_float(r2[1] & 0xffff0000u);
    bf4 b;
#pragma unroll
    for (int j = 0; j < 4; ++j) { b[j] = (__bf16)v[j]; v[j] = (float)b[j]; }
    *(bf4*)(xout + gi) = b;
    write_partial_stats(v, stats, mt * 64 + row, nt, id & 15);
  }
}

extern "C" void kernel_launch(void* const* d_in, const int* in_sizes, int n_in, void* d_out, int out_size, void* d_ws,
                              size_t ws_size, hipStream_t stream) {
  const u16* Ax = (const u16*)d_in[0];
  const u16* wqkv = (const u16*)d_in[1];
  const u16* wo = (const u16*)d_in[2];
  const u16* wg = (const u16*)d_in[4];
  const u16* wu = (const u16*)d_in[5];
  const u16* wd = (const u16*)d_in[6];
  char* ws = (char*)d_ws;
  u16* xa = (u16*)ws;
  u16* xb = (u16*)(ws + 4 * 1024 * 1024);
  u16* qkvp = (u16*)(ws + 8 * 1024 * 1024);
  u16* obuf = (u16*)(ws + 16 * 1024 * 1024);
  u16* hbuf = (u16*)(ws + 20 * 1024 * 1024);
  float2* stats = (float2*)(ws + 30 * 1024 * 1024);
  const size_t smem1 = 64 * ASTR + 2 * 64 * 448;
  const size_t smem2 = 65536;
  const size_t smem3 = 64 * ASTR + 448 * 192;
  const size_t smem4 = 128 * ASTR + 2 * 32 * 576;
  const size_t smem5 = 131072;
  for (int layer = 0; layer < 2; ++layer) {
    const u16* xin = layer == 0 ? Ax : xa;
    if (layer == 0) k_ln_qkv<true><<<224, 768, smem1, stream>>>(xin, wqkv, qkvp, stats);
    else k_ln_qkv<false><<<224, 768, smem1, stream>>>(xin, wqkv, qkvp, stats);
    k_attn<<<256, 1024, smem2, stream>>>(qkvp, obuf);
    k_oproj<<<224, 256, smem3, stream>>>(obuf, wo, xin, xb, stats);
    k_ln_glu<<<224, 512, smem4, stream>>>(xb, wg, wu, hbuf, stats);
    k_down<<<224, 512, smem5, stream>>>(hbuf, wd, xb, layer == 0 ? xa : (u16*)d_out, stats);
  }
}
```

```cpp
#include <hip/hip_runtime.h>
#include <hip/hip_bf16.h>
#include <stdint.h>

constexpr int S_ = 2048, D_ = 448, H_ = 16, DH_ = 28, F_ = 1792;

typedef unsigned short u16;
typedef __attribute__((ext_vector_type(8))) short bf16x8;
typedef __attribute__((ext_vector_type(4))) short s16x4;
typedef __attribute__((ext_vector_type(16))) float f32x16;
typedef __attribute__((ext_vector_type(8))) float f32x8;
typedef __attribute__((ext_vector_type(4))) float f32x4;
typedef __attribute__((ext_vector_type(4))) unsigned u32x4;
typedef __attribute__((ext_vector_type(2))) unsigned u32x2;
typedef __attribute__((ext_vector_type(8))) __bf16 bf8;
typedef __attribute__((ext_vector_type(4))) __bf16 bf4;
typedef __attribute__((ext_vector_type(2))) __bf16 bf2;
typedef __attribute__((address_space(3))) s16x4* lds_s16x4_p;

#define DI __device__ __forceinline__
#define MFMA32(a, b, c) __builtin_amdgcn_mfma_f32_32x32x16_bf16((a), (b), (c), 0, 0, 0)

DI int crow(int reg, int h) { return (reg & 3) + 8 * (reg >> 2) + 4 * h; }

DI s16x4 tr_read(const char* p) {
#if defined(__HIP_DEVICE_COMPILE__)
  return __builtin_amdgcn_ds_read_tr16_b64_v4i16((lds_s16x4_p)(p));
#else
  return s16x4{};
#endif
}
DI bf16x8 cat(s16x4 lo, s16x4 hi) { return __builtin_shufflevector(lo, hi, 0, 1, 2, 3, 4, 5, 6, 7); }
DI bf16x8 pack8(f32x8 v) { return __builtin_bit_cast(bf16x8, __builtin_convertvector(v, bf8)); }
DI u16 f2bf(float f) { return __builtin_bit_cast(u16, (__bf16)f); }
DI float bf2f(u16 u) { return __uint_as_float(((unsigned)u) << 16); }
DI float wave_sum(float v) {
#pragma unroll
  for (int o = 32; o >= 1; o >>= 1) v += __shfl_xor(v, o, 64);
  return v;
}

DI void load8(const float* p, float* v) {
  f32x4 a = *(const f32x4*)p, b = *(const f32x4*)(p + 4);
  v[0] = a[0]; v[1] = a[1]; v[2] = a[2]; v[3] = a[3]; v[4] = b[0]; v[5] = b[1]; v[6] = b[2]; v[7] = b[3];
}
DI void load8(const u16* p, float* v) {
  u32x4 a = *(const u32x4*)p;
#pragma unroll
  for (int i = 0; i < 4; ++i) { v[2 * i] = __uint_as_float(a[i] << 16); v[2 * i + 1] = __uint_as_float(a[i] & 0xffff0000u); }
}

constexpr int ASTR = 912;

template <typename TX, int NROWS, int NW>
DI void ln_rows_to_lds(const TX* __restrict__ x, int row0, char* sA, int wid, int lane) {
  constexpr int RPW = NROWS / NW;
  constexpr int EPP = 16 / sizeof(TX);
  constexpr int NP = 448 / EPP / 4;
  const int rr = lane >> 2, j4 = lane & 3;
#pragma unroll
  for (int pass = 0; pass < RPW / 16; ++pass) {
    const int r = wid * RPW + pass * 16 + rr;
    const TX* src = x + (size_t)(row0 + r) * D_;
    u32x4 raw[NP];
#pragma unroll
    for (int i = 0; i < NP; ++i) raw[i] = *(const u32x4*)(src + (j4 + 4 * i) * EPP);
    float v[NP * EPP];
#pragma unroll
    for (int i = 0; i < NP; ++i) {
      if constexpr (sizeof(TX) == 4) {
#pragma unroll
        for (int e = 0; e < 4; ++e) v[i * 4 + e] = __uint_as_float(raw[i][e]);
      } else {
#pragma unroll
        for (int e = 0; e < 4; ++e) { v[i * 8 + 2 * e] = __uint_as_float(raw[i][e] << 16); v[i * 8 + 2 * e + 1] = __uint_as_float(raw[i][e] & 0xffff0000u); }
      }
    }
    float s = 0.f;
#pragma unroll
    for (int j = 0; j < NP * EPP; ++j) s += v[j];
    s += __shfl_xor(s, 1, 64); s += __shfl_xor(s, 2, 64);
    const float mean = s * (1.f / 448.f);
    float q = 0.f;
#pragma unroll
    for (int j = 0; j < NP * EPP; ++j) { float d = v[j] - mean; q += d * d; }
    q += __shfl_xor(q, 1, 64); q += __shfl_xor(q, 2, 64);
    const float rstd = rsqrtf(q * (1.f / 448.f) + 1e-5f);
    char* dst = sA + r * ASTR;
#pragma unroll
    for (int i = 0; i < NP; ++i) {
      if constexpr (sizeof(TX) == 4) {
        bf4 o;
#pragma unroll
        for (int e = 0; e < 4; ++e) o[e] = (__bf16)((v[i * 4 + e] - mean) * rstd);
        *(bf4*)(dst + (j4 + 4 * i) * 8) = o;
      } else {
        f32x8 o;
#pragma unroll
        for (int e = 0; e < 8; ++e) o[e] = (v[i * 8 + e] - mean) * rstd;
        *(bf16x8*)(dst + (j4 + 4 * i) * 16) = pack8(o);
      }
    }
  }
}

template <int NROWS, int NW>
DI void ln_rows_from_stats(const u16* __restrict__ x, const float2* __restrict__ stats, int row0, char* sA, int wid, int lane) {
  constexpr int RPW = NROWS / NW;
  constexpr int NP = 14;
  const int rr = lane >> 2, j4 = lane & 3;
#pragma unroll
  for (int pass = 0; pass < RPW / 16; ++pass) {
    const int r = wid * RPW + pass * 16 + rr;
    const u16* src = x + (size_t)(row0 + r) * D_;
    u32x4 raw[NP];
#pragma unroll
    for (int i = 0; i < NP; ++i) raw[i] = *(const u32x4*)(src + (j4 + 4 * i) * 8);
    const f32x4* sp = (const f32x4*)(stats + (size_t)(row0 + r) * 8);
    f32x4 s01 = sp[0], s23 = sp[1], s45 = sp[2], s6x = sp[3];
    const float sum = s01[0] + s01[2] + s23[0] + s23[2] + s45[0] + s45[2] + s6x[0];
    const float mean = sum * (1.f / 448.f);
    float m2 = s01[1] + s01[3] + s23[1] + s23[3] + s45[1] + s45[3] + s6x[1];
    float dv = 0.f, t;
    t = s01[0] * (1.f / 64.f) - mean; dv += t * t;
    t = s01[2] * (1.f / 64.f) - mean; dv += t * t;
    t = s23[0] * (1.f / 64.f) - mean; dv += t * t;
    t = s23[2] * (1.f / 64.f) - mean; dv += t * t;
    t = s45[0] * (1.f / 64.f) - mean; dv += t * t;
    t = s45[2] * (1.f / 64.f) - mean; dv += t * t;
    t = s6x[0] * (1.f / 64.f) - mean; dv += t * t;
    m2 += 64.f * dv;
    const float rstd = rsqrtf(m2 * (1.f / 448.f) + 1e-5f);
    const float nb = -mean * rstd;
    char* dst = sA + r * ASTR;
#pragma unroll
    for (int i = 0; i < NP; ++i) {
      f32x8 o;
#pragma unroll
      for (int e = 0; e < 4; ++e) {
        o[2 * e] = __uint_as_float(raw[i][e] << 16) * rstd + nb;
        o[2 * e + 1] = __uint_as_float(raw[i][e] & 0xffff0000u) * rstd + nb;
      }
      *(bf16x8*)(dst + (j4 + 4 * i) * 16) = pack8(o);
    }
  }
}

DI void write_partial_stats(f32x4 v, float2* __restrict__ stats, int grow, int nt, int c16) {
  float s = v[0] + v[1] + v[2] + v[3];
  s += __shfl_xor(s, 1, 64); s += __shfl_xor(s, 2, 64); s += __shfl_xor(s, 4, 64); s += __shfl_xor(s, 8, 64);
  const float mp = s * (1.f / 64.f);
  float q = (v[0] - mp) * (v[0] - mp) + (v[1] - mp) * (v[1] - mp) + (v[2] - mp) * (v[2] - mp) + (v[3] - mp) * (v[3] - mp);
  q += __shfl_xor(q, 1, 64); q += __shfl_xor(q, 2, 64); q += __shfl_xor(q, 4, 64); q += __shfl_xor(q, 8, 64);
  if (c16 == 0) stats[(size_t)grow * 8 + nt] = make_float2(s, q);
}

template <bool FIRST>
__global__ __launch_bounds__(768) void k_ln_qkv(const u16* __restrict__ x, const u16* __restrict__ w, u16* __restrict__ qkvp, const float2* __restrict__ stats) {
  extern __shared__ __attribute__((aligned(16))) char smem[];
  constexpr int BSTR = 448;
  char* sA = smem;
  char* sB = smem + 64 * ASTR;
  const int tid = threadIdx.x, wid = tid >> 6, lane = tid & 63;
  const int xcd_ = blockIdx.x & 7, idx_ = blockIdx.x >> 3;
  const int mt = xcd_ * 4 + idx_ / 7, nt = idx_ % 7;
  const int r32 = lane & 31, h = lane >> 5;
  const int wm = wid & 1, wn = wid >> 1;

  u32x4 st[2][2];
  auto gload = [&](int kc, u32x4* stp) {
#pragma unroll
    for (int i = 0; i < 2; ++i) {
      int p = tid + 768 * i; int row = p / 24, c = p % 24;
      stp[i] = *(const u32x4*)(w + (size_t)(kc * 64 + row) * 1344 + nt * 192 + c * 8);
    }
  };
  auto swrite = [&](int buf, const u32x4* stp) {
#pragma unroll
    for (int i = 0; i < 2; ++i) {
      int p = tid + 768 * i; int row = p / 24, c = p % 24;
      *(u32x4*)(sB + buf * (64 * BSTR) + row * BSTR + c * 16) = stp[i];
    }
  };
  gload(0, st[0]); gload(1, st[1]);
  if (wid < 4) {
    if constexpr (FIRST) ln_rows_to_lds<u16, 64, 4>(x, mt * 64, sA, wid, lane);
    else ln_rows_from_stats<64, 4>(x, stats, mt * 64, sA, wid, lane);
  }
  swrite(0, st[0]);
  __syncthreads();

  f32x16 acc;
#pragma unroll
  for (int i = 0; i < 16; ++i) acc[i] = 0.f;

  const int i16 = lane & 15, q = i16 >> 2, p4 = i16 & 3, blk = (lane >> 4) & 1;
  const int boff = (8 * h + q) * BSTR + (wn * 32 + 16 * blk + 4 * p4) * 2;
  const int aoff = (wm * 32 + r32) * ASTR + 16 * h;
#pragma unroll
  for (int kc = 0; kc < 7; ++kc) {
    if (kc + 2 < 7) gload(kc + 2, st[kc & 1]);
    const char* bb = sB + (kc & 1) * (64 * BSTR) + boff;
#pragma unroll
    for (int ks = 0; ks < 4; ++ks) {
      bf16x8 a = *(const bf16x8*)(sA + aoff + (kc * 64 + ks * 16) * 2);
      s16x4 lo = tr_read(bb + ks * 16 * BSTR);
      s16x4 hi = tr_read(bb + ks * 16 * BSTR + 4 * BSTR);
      acc = MFMA32(a, cat(lo, hi), acc);
    }
    if (kc + 1 < 7) swrite((kc + 1) & 1, st[(kc + 1) & 1]);
    __syncthreads();
  }
  constexpr int CSTR = 400;
  char* sC = smem;
  const float SC = 0.18898223650461362f * 1.4426950408889634f;
  {
    const int col = wn * 32 + r32;
    const float sc = (nt * 192 + col) < 448 ? SC : 1.f;
#pragma unroll
    for (int i = 0; i < 16; ++i) {
      const int row = wm * 32 + crow(i, h);
      *(u16*)(sC + row * CSTR + col * 2) = f2bf(acc[i] * sc);
    }
  }
  __syncthreads();
#pragma unroll
  for (int i = 0; i < 4; ++i) {
    const int id = tid + 768 * i; const int row = id / 48, pc = id % 48;
    const int n = nt * 192 + pc * 4;
    const int sec = n / 448, rem = n % 448, head = rem / 28, d0 = rem % 28;
    u32x2 v = *(const u32x2*)(sC + row * CSTR + pc * 8);
    u16* dst = qkvp + ((size_t)(sec * 16 + head) * 2048 + (mt * 64 + row)) * 32 + d0;
    *(u32x2*)dst = v;
    if (d0 == 24) {
      u32x2 pad; pad[0] = (sec >= 1) ? 0x00003F80u : 0u; pad[1] = 0u;
      *(u32x2*)(dst + 4) = pad;
    }
  }
}

constexpr float RTHR = 8.f;
DI float max3f(float a, float b, float c) { return __builtin_elementwise_maximum(__builtin_elementwise_maximum(a, b), c); }
__global__ __launch_bounds__(1024) void k_attn(const u16* __restrict__ qkvp, u16* __restrict__ o_out) {
  extern __shared__ __attribute__((aligned(16))) char smem[];
  const int tid = threadIdx.x, wid = tid >> 6, lane = tid & 63;
  const int xcd_ = blockIdx.x & 7, idx_ = blockIdx.x >> 3;
  const int head = xcd_ * 2 + (idx_ >> 4), qb = idx_ & 15;
  const int qt = wid & 3, kq = wid >> 2;
  const int r32 = lane & 31, h = lane >> 5;
  const u16* Q = qkvp + (size_t)(head) * 2048 * 32;
  const u16* K = qkvp + (size_t)(16 + head) * 2048 * 32;
  const u16* V = qkvp + (size_t)(32 + head) * 2048 * 32;
  const int qrow = qb * 128 + qt * 32 + r32;
  const bf16x8 q0 = *(const bf16x8*)(Q + (size_t)qrow * 32 + 8 * h);
  bf16x8 q1 = *(const bf16x8*)(Q + (size_t)qrow * 32 + 16 + 8 * h);
  const int squart = tid >> 8, st = tid & 255, skey = st >> 2, sslot = st & 3;
  const u16* gK = K + (size_t)(squart * 512 + skey) * 32 + sslot * 8;
  const u16* gV = V + (size_t)(squart * 512 + skey) * 32 + sslot * 8;
  char* sbase_st = smem + squart * 16384;
  const int kst_off = skey * 64 + ((sslot ^ ((skey >> 2) & 3)) * 16);
  const int vst_off = 4096 + skey * 64 + sslot * 16;
  u32x4 rg[2][2];
  auto gload = [&](int it, u32x4* r) {
    r[0] = *(const u32x4*)(gK + (size_t)it * 64 * 32);
    r[1] = *(const u32x4*)(gV + (size_t)it * 64 * 32);
  };
  auto swrite = [&](int buf, const u32x4* r) {
    char* sw = sbase_st + buf * 8192;
    *(u32x4*)(sw + kst_off) = r[0];
    *(u32x4*)(sw + vst_off) = r[1];
  };
  gload(0, rg[0]); gload(1, rg[1]);
  swrite(0, rg[0]);
  __syncthreads();

  const char* sbase = smem + kq * 16384;
  const int swz = (r32 >> 2) & 3;
  const int ka_off0 = r32 * 64 + ((h ^ swz) * 16);
  const int ka_off1 = r32 * 64 + (((2 + h) ^ swz) * 16);
  const int i16 = lane & 15, q4 = i16 >> 2, p4 = i16 & 3, blk = (lane >> 4) & 1;
  const int v_off = 4096 + (4 * h + q4) * 64 + (16 * blk + 4 * p4) * 2;

  f32x16 o, zero;
#pragma unroll
  for (int i = 0; i < 16; ++i) { o[i] = 0.f; zero[i] = 0.f; }
  float m = 0.f;
#pragma unroll
  for (int it = 0; it < 8; ++it) {
    if (it + 2 < 8) gload(it + 2, rg[it & 1]);
    {
      const char* sb = sbase + (it & 1) * 8192;
      bf16x8 ka0 = *(const bf16x8*)(sb + ka_off0);
      bf16x8 ka1 = *(const bf16x8*)(sb + ka_off1);
      bf16x8 kb0 = *(const bf16x8*)(sb + 2048 + ka_off0);
      bf16x8 kb1 = *(const bf16x8*)(sb + 2048 + ka_off1);
      f32x16 s0 = MFMA32(ka0, q0, zero); s0 = MFMA32(ka1, q1, s0);
      f32x16 s1 = MFMA32(kb0, q0, zero); s1 = MFMA32(kb1, q1, s1);
      float mx = __builtin_elementwise_maximum(s0[0], s1[0]);
#pragma unroll
      for (int i = 1; i < 16; ++i) mx = max3f(mx, s0[i], s1[i]);
      {
        auto rr = __builtin_amdgcn_permlane32_swap(__float_as_uint(mx), __float_as_uint(mx), false, false);
        mx = __builtin_elementwise_maximum(__uint_as_float(rr[0]), __uint_as_float(rr[1]));
      }
      const bool first = (it == 0);
      if (first || __any(mx > RTHR)) {
        const float t = first ? mx : __builtin_elementwise_maximum(mx, 0.f);
        const u16 mb = f2bf(m + t);
        const float mnew = bf2f(mb);
        const float d = mnew - m;
        const float alpha = first ? 1.f : __builtin_amdgcn_exp2f(-d);
#pragma unroll
        for (int i = 0; i < 16; ++i) { o[i] *= alpha; s0[i] -= d; s1[i] -= d; }
        m = mnew;
        if (h == 1) q1[4] = (short)(mb ^ 0x8000u);
      }
      f32x8 e0, e1, e2, e3;
#pragma unroll
      for (int i = 0; i < 8; ++i) {
        e0[i] = __builtin_amdgcn_exp2f(s0[i]); e1[i] = __builtin_amdgcn_exp2f(s0[8 + i]);
        e2[i] = __builtin_amdgcn_exp2f(s1[i]); e3[i] = __builtin_amdgcn_exp2f(s1[8 + i]);
      }
      bf16x8 p0 = pack8(e0), p1 = pack8(e1), p2 = pack8(e2), p3 = pack8(e3);
      const char* vb = sb + v_off;
      o = MFMA32(cat(tr_read(vb), tr_read(vb + 8 * 64)), p0, o);
      o = MFMA32(cat(tr_read(vb + 16 * 64), tr_read(vb + 24 * 64)), p1, o);
      o = MFMA32(cat(tr_read(vb + 32 * 64), tr_read(vb + 40 * 64)), p2, o);
      o = MFMA32(cat(tr_read(vb + 48 * 64), tr_read(vb + 56 * 64)), p3, o);
    }
    if (it + 1 < 8) swrite((it + 1) & 1, rg[(it + 1) & 1]);
    __syncthreads();
  }
  float* sM = (float*)smem;
  if (kq != 0) {
    float* dst = sM + ((kq - 1) * 4 + qt) * 17 * 64 + lane;
    dst[0] = m;
#pragma unroll
    for (int i = 0; i < 16; ++i) dst[(i + 1) * 64] = o[i];
  }
  __syncthreads();
  if (kq == 0) {
#pragma unroll
    for (int j = 0; j < 3; ++j) {
      const float* src = sM + (j * 4 + qt) * 17 * 64 + lane;
      const float m2 = src[0];
      const float mn = __builtin_elementwise_maximum(m, m2);
      const float a1 = __builtin_amdgcn_exp2f(m - mn), a2 = __builtin_amdgcn_exp2f(m2 - mn);
#pragma unroll
      for (int i = 0; i < 16; ++i) o[i] = o[i] * a1 + src[(i + 1) * 64] * a2;
      m = mn;
    }
    float l = o[12];
    {
      auto rr = __builtin_amdgcn_permlane32_swap(__float_as_uint(l), __float_as_uint(l), false, false);
      l = (h == 0) ? __uint_as_float(rr[1]) : l;
    }
    const float inv = 1.f / l;
    u16* dst = o_out + (size_t)qrow * 448 + head * 28;
#pragma unroll
    for (int g = 0; g < 4; ++g) {
      const int d0 = 8 * g + 4 * h;
      if (d0 < 28) {
        bf4 v;
#pragma unroll
        for (int j = 0; j < 4; ++j) v[j] = (__bf16)(o[4 * g + j] * inv);
        *(bf4*)(dst + d0) = v;
      }
    }
  }
}

__global__ __launch_bounds__(256) void k_oproj(const u16* __restrict__ A, const u16* __restrict__ w, const u16* __restrict__ xin,
                                               u16* __restrict__ xout, float2* __restrict__ stats) {
  extern __shared__ __attribute__((aligned(16))) char smem[];
  constexpr int BSTR = 192;
  char* sA = smem;
  char* sB = smem + 64 * ASTR;
  const int tid = threadIdx.x, wid = tid >> 6, lane = tid & 63;
  const int xcd_ = blockIdx.x & 7, idx_ = blockIdx.x >> 3;
  const int mt = xcd_ * 4 + idx_ / 7, nt = idx_ % 7;
  const int r32 = lane & 31, h = lane >> 5;
  const int wm = wid & 1, wn = wid >> 1;
  {
    u32x4 ra[14], rb[14];
#pragma unroll
    for (int i = 0; i < 14; ++i) {
      int p = tid + 256 * i; int row = p / 56, c = p % 56;
      ra[i] = *(const u32x4*)(A + (size_t)(mt * 64 + row) * 448 + c * 8);
    }
#pragma unroll
    for (int i = 0; i < 14; ++i) {
      int p = tid + 256 * i; int row = p >> 3, c = p & 7;
      rb[i] = *(const u32x4*)(w + (size_t)row * 448 + nt * 64 + c * 8);
    }
#pragma unroll
    for (int i = 0; i < 14; ++i) {
      int p = tid + 256 * i; int row = p / 56, c = p % 56;
      *(u32x4*)(sA + row * ASTR + c * 16) = ra[i];
    }
#pragma unroll
    for (int i = 0; i < 14; ++i) {
      int p = tid + 256 * i; int row = p >> 3, c = p & 7;
      *(u32x4*)(sB + row * BSTR + c * 16) = rb[i];
    }
  }
  __syncthreads();
  f32x16 acc;
#pragma unroll
  for (int i = 0; i < 16; ++i) acc[i] = 0.f;
  const int i16 = lane & 15, q = i16 >> 2, p4 = i16 & 3, blk = (lane >> 4) & 1;
  const int boff = (8 * h + q) * BSTR + (wn * 32 + 16 * blk + 4 * p4) * 2;
  const int aoff = (wm * 32 + r32) * ASTR + 16 * h;
#pragma unroll 4
  for (int ks = 0; ks < 28; ++ks) {
    bf16x8 a = *(const bf16x8*)(sA + aoff + ks * 32);
    s16x4 lo = tr_read(sB + boff + ks * 16 * BSTR);
    s16x4 hi = tr_read(sB + boff + ks * 16 * BSTR + 4 * BSTR);
    acc = MFMA32(a, cat(lo, hi), acc);
  }
  __syncthreads();
  float* sR = (float*)smem;
#pragma unroll
  for (int i = 0; i < 16; ++i) sR[(wm * 32 + crow(i, h)) * 64 + wn * 32 + r32] = acc[i];
  __syncthreads();
#pragma unroll
  for (int i = 0; i < 4; ++i) {
    const int id = tid + 256 * i; const int row = id >> 4, c16 = id & 15, c4 = c16 * 4;
    const size_t gi = (size_t)(mt * 64 + row) * 448 + nt * 64 + c4;
    f32x4 xi;
    {
      u32x2 r2 = *(const u32x2*)(xin + gi);
      xi[0] = __uint_as_float(r2[0] << 16); xi[1] = __uint_as_float(r2[0] & 0xffff0000u);
      xi[2] = __uint_as_float(r2[1] << 16); xi[3] = __uint_as_float(r2[1] & 0xffff0000u);
    }
    f32x4 v = *(const f32x4*)(sR + row * 64 + c4) + xi;
    bf4 b;
#pragma unroll
    for (int j = 0; j < 4; ++j) { b[j] = (__bf16)v[j]; v[j] = (float)b[j]; }
    *(bf4*)(xout + gi) = b;
    write_partial_stats(v, stats, mt * 64 + row, nt, c16);
  }
}

__global__ __launch_bounds__(512) void k_ln_glu(const u16* __restrict__ x, const u16* __restrict__ wg, const u16* __restrict__ wu,
                                                u16* __restrict__ hout, const float2* __restrict__ stats) {
  extern __shared__ __attribute__((aligned(16))) char smem[];
  constexpr int BSTR = 576;
  char* sA = smem;
  char* sB = smem + 128 * ASTR;
  const int tid = threadIdx.x, wid = tid >> 6, lane = tid & 63;
  const int xcd_ = blockIdx.x & 7, idx_ = blockIdx.x >> 3;
  const int mt = (xcd_ >> 1) * 4 + idx_ / 7, ft = (xcd_ & 1) * 7 + idx_ % 7;
  const int r32 = lane & 31, h = lane >> 5;
  const int wm = wid & 3, wn = wid >> 2;
  u32x4 st[2][2];
  auto gload = [&](int kc, u32x4* stp) {
#pragma unroll
    for (int i = 0; i < 2; ++i) {
      int p = tid + 512 * i; int row = p >> 5, c = p & 31;
      const u16* src = (c < 16) ? wg : wu;
      stp[i] = *(const u32x4*)(src + (size_t)(kc * 32 + row) * 1792 + ft * 128 + (c & 15) * 8);
    }
  };
  auto swrite = [&](int buf, const u32x4* stp) {
#pragma unroll
    for (int i = 0; i < 2; ++i) {
      int p = tid + 512 * i; int row = p >> 5, c = p & 31;
      *(u32x4*)(sB + buf * (32 * BSTR) + row * BSTR + c * 16) = stp[i];
    }
  };
  gload(0, st[0]); gload(1, st[1]);
  ln_rows_from_stats<128, 8>(x, stats, mt * 128, sA, wid, lane);
  swrite(0, st[0]);
  __syncthreads();
  f32x16 acc[4];
#pragma unroll
  for (int b = 0; b < 4; ++b)
#pragma unroll
    for (int i = 0; i < 16; ++i) acc[b][i] = 0.f;
  const int i16 = lane & 15, q = i16 >> 2, p4 = i16 & 3, blk = (lane >> 4) & 1;
  const int boff = (8 * h + q) * BSTR + (wn * 64 + 16 * blk + 4 * p4) * 2;
  const int aoff = (wm * 32 + r32) * ASTR + 16 * h;
#pragma unroll
  for (int kc = 0; kc < 14; ++kc) {
    if (kc + 2 < 14) gload(kc + 2, st[kc & 1]);
    const char* bb = sB + (kc & 1) * (32 * BSTR) + boff;
#pragma unroll
    for (int ks = 0; ks < 2; ++ks) {
      bf16x8 a0 = *(const bf16x8*)(sA + aoff + (kc * 32 + ks * 16) * 2);
#pragma unroll
      for (int t = 0; t < 4; ++t) {
        const int co = (t & 1) * 64 + (t >> 1) * 256;
        s16x4 lo = tr_read(bb + ks * 16 * BSTR + co);
        s16x4 hi = tr_read(bb + ks * 16 * BSTR + 4 * BSTR + co);
        acc[t] = MFMA32(a0, cat(lo, hi), acc[t]);
      }
    }
    if (kc + 1 < 14) swrite((kc + 1) & 1, st[(kc + 1) & 1]);
    __syncthreads();
  }
  constexpr int CSTR = 272;
  char* sC = smem;
#pragma unroll
  for (int t = 0; t < 2; ++t) {
    const int col = wn * 64 + t * 32 + r32;
#pragma unroll
    for (int i = 0; i < 16; ++i) {
      const int row = wm * 32 + crow(i, h);
      const float g = acc[t][i], u = acc[2 + t][i];
      const float sg = 1.f / (1.f + __expf(-g));
      *(u16*)(sC + row * CSTR + col * 2) = f2bf(sg * u);
    }
  }
  __syncthreads();
#pragma unroll
  for (int i = 0; i < 4; ++i) {
    const int id = tid + 512 * i; const int row = id >> 4, c = id & 15;
    u32x4 v = *(const u32x4*)(sC + row * CSTR + c * 16);
    *(u32x4*)(hout + (size_t)(mt * 128 + row) * 1792 + ft * 128 + c * 8) = v;
  }
}

__global__ __launch_bounds__(512) void k_down(const u16* __restrict__ hin, const u16* __restrict__ w, const u16* __restrict__ xin,
                                              u16* __restrict__ xout, float2* __restrict__ stats) {
  extern __shared__ __attribute__((aligned(16))) char smem[];
  constexpr int A2STR = 272;
  constexpr int BSTR = 192;
  constexpr int ABUF = 64 * A2STR, BBUF = 128 * BSTR;
  char* sA = smem;
  char* sB = smem + 2 * ABUF;
  const int tid = threadIdx.x, wid = tid >> 6, lane = tid & 63;
  const int xcd_ = blockIdx.x & 7, idx_ = blockIdx.x >> 3;
  const int mt = xcd_ * 4 + idx_ / 7, nt = idx_ % 7;
  const int r32 = lane & 31, h = lane >> 5;
  u32x4 sta[2][2], stb[2][2];
  auto gload = [&](int kc, u32x4* pa, u32x4* pb) {
#pragma unroll
    for (int i = 0; i < 2; ++i) {
      int p = tid + 512 * i; int row = p >> 4, c = p & 15;
      pa[i] = *(const u32x4*)(hin + (size_t)(mt * 64 + row) * 1792 + kc * 128 + c * 8);
    }
#pragma unroll
    for (int i = 0; i < 2; ++i) {
      int p = tid + 512 * i; int row = p >> 3, c = p & 7;
      pb[i] = *(const u32x4*)(w + (size_t)(kc * 128 + row) * 448 + nt * 64 + c * 8);
    }
  };
  auto swrite = [&](int buf, const u32x4* pa, const u32x4* pb) {
#pragma unroll
    for (int i = 0; i < 2; ++i) {
      int p = tid + 512 * i; int row = p >> 4, c = p & 15;
      *(u32x4*)(sA + buf * ABUF + row * A2STR + c * 16) = pa[i];
    }
#pragma unroll
    for (int i = 0; i < 2; ++i) {
      int p = tid + 512 * i; int row = p >> 3, c = p & 7;
      *(u32x4*)(sB + buf * BBUF + row * BSTR + c * 16) = pb[i];
    }
  };
  gload(0, sta[0], stb[0]); gload(1, sta[1], stb[1]);
  swrite(0, sta[0], stb[0]);
  __syncthreads();
  f32x16 acc[2][2];
#pragma unroll
  for (int a = 0; a < 2; ++a)
#pragma unroll
    for (int b = 0; b < 2; ++b)
#pragma unroll
      for (int i = 0; i < 16; ++i) acc[a][b][i] = 0.f;
  const int i16 = lane & 15, q = i16 >> 2, p4 = i16 & 3, blk = (lane >> 4) & 1;
  const int boff = (wid * 16 + 8 * h + q) * BSTR + (16 * blk + 4 * p4) * 2;
  const int aoff = r32 * A2STR + wid * 32 + 16 * h;
#pragma unroll
  for (int kc = 0; kc < 14; ++kc) {
    if (kc + 2 < 14) gload(kc + 2, sta[kc & 1], stb[kc & 1]);
    const char* ab = sA + (kc & 1) * ABUF + aoff;
    const char* bb = sB + (kc & 1) * BBUF + boff;
    {
      bf16x8 a0 = *(const bf16x8*)(ab);
      bf16x8 a1 = *(const bf16x8*)(ab + 32 * A2STR);
#pragma unroll
      for (int t = 0; t < 2; ++t) {
        s16x4 lo = tr_read(bb + t * 64);
        s16x4 hi = tr_read(bb + 4 * BSTR + t * 64);
        bf16x8 b = cat(lo, hi);
        acc[0][t] = MFMA32(a0, b, acc[0][t]);
        acc[1][t] = MFMA32(a1, b, acc[1][t]);
      }
    }
    if (kc + 1 < 14) swrite((kc + 1) & 1, sta[(kc + 1) & 1], stb[(kc + 1) & 1]);
    __syncthreads();
  }
  float* sR = (float*)smem;
#pragma unroll
  for (int a = 0; a < 2; ++a)
#pragma unroll
    for (int t = 0; t < 2; ++t)
#pragma unroll
      for (int i = 0; i < 16; ++i) {
        const int row = a * 32 + crow(i, h), col = t * 32 + r32;
        sR[wid * 4096 + row * 64 + col] = acc[a][t][i];
      }
  __syncthreads();
#pragma unroll
  for (int i = 0; i < 2; ++i) {
    const int id = tid + 512 * i; const int row = id >> 4, c4 = (id & 15) * 4;
    const size_t gi = (size_t)(mt * 64 + row) * 448 + nt * 64 + c4;
    u32x2 r2 = *(const u32x2*)(xin + gi);
    f32x4 v = *(const f32x4*)(sR + row * 64 + c4);
#pragma unroll
    for (int w2 = 1; w2 < 8; ++w2) v += *(const f32x4*)(sR + w2 * 4096 + row * 64 + c4);
    v[0] += __uint_as_float(r2[0] << 16); v[1] += __uint_as_float(r2[0] & 0xffff0000u);
    v[2] += __uint_as_float(r2[1] << 16); v[3] += __uint_as_float(r2[1] & 0xffff0000u);
    bf4 b;
#pragma unroll
    for (int j = 0; j < 4; ++j) { b[j] = (__bf16)v[j]; v[j] = (float)b[j]; }
    *(bf4*)(xout + gi) = b;
    write_partial_stats(v, stats, mt * 64 + row, nt, id & 15);
  }
}

extern "C" void kernel_launch(void* const* d_in, const int* in_sizes, int n_in, void* d_out, int out_size, void* d_ws,
                              size_t ws_size, hipStream_t stream) {
  const u16* Ax = (const u16*)d_in[0];
  const u16* wqkv = (const u16*)d_in[1];
  const u16* wo = (const u16*)d_in[2];
  const u16* wg = (const u16*)d_in[4];
  const u16* wu = (const u16*)d_in[5];
  const u16* wd = (const u16*)d_in[6];
  char* ws = (char*)d_ws;
  u16* xa = (u16*)ws;
  u16* xb = (u16*)(ws + 4 * 1024 * 1024);
  u16* qkvp = (u16*)(ws + 8 * 1024 * 1024);
  u16* obuf = (u16*)(ws + 16 * 1024 * 1024);
  u16* hbuf = (u16*)(ws + 20 * 1024 * 1024);
  float2* stats = (float2*)(ws + 30 * 1024 * 1024);
  const size_t smem1 = 64 * ASTR + 2 * 64 * 448;
  const size_t smem2 = 65536;
  const size_t smem3 = 64 * ASTR + 448 * 192;
  const size_t smem4 = 128 * ASTR + 2 * 32 * 576;
  const size_t smem5 = 131072;
  for (int layer = 0; layer < 2; ++layer) {
    const u16* xin = layer == 0 ? Ax : xa;
    if (layer == 0) k_ln_qkv<true><<<224, 768, smem1, stream>>>(xin, wqkv, qkvp, stats);
    else k_ln_qkv<false><<<224, 768, smem1, stream>>>(xin, wqkv, qkvp, stats);
    k_attn<<<256, 1024, smem2, stream>>>(qkvp, obuf);
    k_oproj<<<224, 256, smem3, stream>>>(obuf, wo, xin, xb, stats);
    k_ln_glu<<<224, 512, smem4, stream>>>(xb, wg, wu, hbuf, stats);
    k_down<<<224, 512, smem5, stream>>>(hbuf, wd, xb, layer == 0 ? xa : (u16*)d_out, stats);
  }
}
```
